```python
import torch

def _softmax(x, axis=-1):
    return torch.softmax(x, dim=axis)
def _gelu(x):
    return torch.nn.functional.gelu(x, approximate="tanh")
def _silu(x):
    return torch.nn.functional.silu(x)
def _sigmoid(x):
    return torch.sigmoid(x)
def _layernorm(x, axis=-1, eps=1e-5):
    m = x.mean(dim=axis, keepdim=True)
    v = x.var(dim=axis, keepdim=True, unbiased=False)
    return (x - m) / torch.sqrt(v + eps)
def _scatter_add(tgt, idx, src, axis=0):
    return tgt.clone().index_add_(axis, idx, src)
def _segment_sum(src, seg, n):
    out = torch.zeros((n,) + src.shape[1:], dtype=src.dtype)
    return out.index_add_(0, seg, src)
def _pad(x, pw):
    flat = tuple(v for pair in reversed(pw) for v in pair)
    return torch.nn.functional.pad(x, flat)

kernel_name = 'standalone__spectral_conv_chebnet__graph'

def setup_inputs():

    _w = (lambda a: (lambda t: t.to(torch.bfloat16) if t.is_floating_point() else t)(torch.as_tensor(a)))
    N, deg, D, K = 50000, 16, 64, 3
    E = N * deg
    src = (lambda lo, hi, *s: torch.randint(lo, hi, s))(0, N, E).to(torch.int64)
    dst = (lambda lo, hi, *s: torch.randint(lo, hi, s))(0, N, E).to(torch.int64)
    deg_cnt = torch.bincount(dst, minlength=N).to(float) + 1.0
    dis = (1.0 / (lambda _x: torch.sqrt(torch.as_tensor(_x)))(deg_cnt)).to(float)
    ew = -(dis[src] * dis[dst])
    return {
        "x": _w((lambda *s: torch.randn(*s, dtype=torch.bfloat16) * 0.05)(N, D)),
        "src": _w(src),
        "dst": _w(dst),
        "ew": _w(ew),
        "w": _w((lambda *s: torch.randn(*s, dtype=torch.bfloat16) * 0.05)(K, D, D) * 0.2),
    }

def reference(x, src, dst, ew, w):
    N, K = x.shape[0], w.shape[0]
    Ts = [x]
    if K > 1:
        Ts.append(_segment_sum(ew[:, None] * x[src], dst, N))
    for k in range(2, K):
        Tk = 2.0 * _segment_sum(ew[:, None] * Ts[-1][src], dst, N) - Ts[-2]
        Ts.append(Tk)
    T = torch.stack(Ts, 0)
    return torch.einsum('knd,kde->ne', T, w)
```

```cpp
#include <hip/hip_runtime.h>
#include <hip/hip_bf16.h>
#include <stdint.h>

typedef unsigned u32;
typedef u32 u32x4 __attribute__((ext_vector_type(4)));
typedef u32 u32x2 __attribute__((ext_vector_type(2)));
typedef float f32x4 __attribute__((ext_vector_type(4)));
typedef float f32x2 __attribute__((ext_vector_type(2)));
typedef __bf16 bf16x8 __attribute__((ext_vector_type(8)));
typedef __bf16 bf16x2 __attribute__((ext_vector_type(2)));
typedef short s16x4 __attribute__((ext_vector_type(4)));
typedef __attribute__((address_space(3))) void* lds_vptr;
typedef __attribute__((address_space(3))) s16x4* lds_s16x4ptr;

constexpr int NB = 256;
constexpr int NP = 256;
constexpr int WPB = 16;
constexpr int TPB = WPB * 64;
constexpr int VB_LOG = 12;
constexpr int NPH = 8;
constexpr int CHUNKB = 192;
constexpr int LIST_HDR = 128;
constexpr int LIST_BYTES = 2048;
constexpr int LIST_CAP = 320;
constexpr int MAXCH = 4096;

constexpr int L_STAGE = 0;
constexpr int L_LISTS = L_STAGE + WPB * 4096;
constexpr int L_HIST = L_LISTS + WPB * LIST_BYTES;
constexpr int L_BASE = L_HIST + 512;
constexpr int L_FLAG = L_BASE + 512;
constexpr int L_PRE = L_FLAG + 16;
constexpr int L_CI = L_PRE + 1040;
constexpr int L_WFRAG = L_CI + 1024;
constexpr int L_TOTAL1 = L_WFRAG;
constexpr int L_TOTAL2 = L_WFRAG + 24 * 1024;

__device__ __forceinline__ __amdgpu_buffer_rsrc_t mk_rsrc(const void* p, u32 bytes) {
  return __builtin_amdgcn_make_buffer_rsrc((void*)p, 0, bytes, 0x00020000);
}
__device__ __forceinline__ u32 pk_bf16(float a, float b) {
  f32x2 v = {a, b};
  return __builtin_bit_cast(u32, __builtin_convertvector(v, bf16x2));
}
__device__ __forceinline__ float bf_lo(u32 v) { return __uint_as_float(v << 16); }
__device__ __forceinline__ float bf_hi(u32 v) { return __uint_as_float(v & 0xffff0000u); }

__global__ __launch_bounds__(TPB) void k_part(const int* __restrict__ src, const int* __restrict__ dst,
                                              const unsigned short* __restrict__ ew, const unsigned short* __restrict__ w,
                                              u32x2* __restrict__ part, u32* __restrict__ cellinfo,
                                              unsigned short* __restrict__ wfrag, int N, int E, int CH, u32 magic) {
  __shared__ __attribute__((aligned(16))) u32 s_hist[NB];
  __shared__ __attribute__((aligned(16))) u32 s_base[NB];
  __shared__ __attribute__((aligned(16))) u32x2 s_ent[MAXCH];
  const int tid = threadIdx.x, p = blockIdx.x;
  const int e0 = p * CH;
  int n = E - e0; n = n < 0 ? 0 : (n > CH ? CH : n);
  const int i0 = tid * 4;
  u32 d[4], s[4], wv[4];
  if (i0 + 3 < n) {
    u32x4 d4 = *(const u32x4*)(dst + e0 + i0);
    u32x4 s4 = *(const u32x4*)(src + e0 + i0);
    u32x2 w4 = *(const u32x2*)(ew + e0 + i0);
    d[0] = d4[0]; d[1] = d4[1]; d[2] = d4[2]; d[3] = d4[3];
    s[0] = s4[0]; s[1] = s4[1]; s[2] = s4[2]; s[3] = s4[3];
    wv[0] = w4[0] & 0xffffu; wv[1] = w4[0] >> 16; wv[2] = w4[1] & 0xffffu; wv[3] = w4[1] >> 16;
  } else {
#pragma unroll
    for (int k = 0; k < 4; k++) {
      d[k] = 0; s[k] = 0; wv[k] = 0;
      if (i0 + k < n) { d[k] = dst[e0 + i0 + k]; s[k] = src[e0 + i0 + k]; wv[k] = ew[e0 + i0 + k]; }
    }
  }
  if (tid < NB) s_hist[tid] = 0;
  if (p < 12) {
    const int o = p * 1024 + tid;
    const int j = o & 7, l = (o >> 3) & 63, u = (o >> 9) & 3, ss = (o >> 11) & 1, k = o >> 12;
    const int i = l & 15, q = l >> 4;
    const int e = 16 * (i >> 2) + 4 * u + (i & 3);
    const int dd = (k == 1) ? (16 * (2 * ss + (j >> 2)) + 4 * q + (j & 3)) : (16 * q + 8 * ss + j);
    wfrag[o] = w[k * 4096 + dd * 64 + e];
  }
  __syncthreads();
  u32 bk[4], rk[4], hi[4];
#pragma unroll
  for (int k = 0; k < 4; k++) {
    const u32 v = __umulhi(d[k], magic);
    const u32 st = (v * (u32)N + ((1u << VB_LOG) - 1)) >> VB_LOG;
    const u32 dl = d[k] - st;
    bk[k] = v >> 4;
    hi[k] = ((v & 15u) << 3) | (s[k] >> 13) | (0x10000u << dl);
    rk[k] = 0;
    if (i0 + k < n) rk[k] = atomicAdd(&s_hist[bk[k]], 1u);
  }
  __syncthreads();
  if (tid < 64) {
    u32x4 h = *(const u32x4*)(s_hist + tid * 4);
    const u32 t = h[0] + h[1] + h[2] + h[3];
    u32 inc = t;
#pragma unroll
    for (int o = 1; o < 64; o <<= 1) { u32 y = __shfl_up(inc, o, 64); if (tid >= o) inc += y; }
    const u32 ex = inc - t;
    u32x4 bs = {ex, ex + h[0], ex + h[0] + h[1], ex + h[0] + h[1] + h[2]};
    *(u32x4*)(s_base + tid * 4) = bs;
  }
  __syncthreads();
#pragma unroll
  for (int k = 0; k < 4; k++) {
    if (i0 + k < n) {
      const u32 pos = s_base[bk[k]] + rk[k];
      s_ent[pos] = u32x2{s[k] | (wv[k] << 16), hi[k]};
    }
  }
  if (tid < NB) cellinfo[tid * NP + p] = s_base[tid] | (s_hist[tid] << 16);
  __syncthreads();
  u32x4* po = (u32x4*)(part + (size_t)p * CH);
  const u32x4* se = (const u32x4*)s_ent;
  const int n2 = (n + 1) >> 1;
  for (int i = tid; i < n2; i += TPB) po[i] = se[i];
}

struct LaneC {
  u32 loff0, loff1;
  u32 tr0;
  u32 g8;
  u32 q32;
  u32 nsh;
};

__device__ __forceinline__ void issue_chunk(char* smem, int stage_off, __amdgpu_buffer_rsrc_t rs, u32x2 st, int rem,
                                            const LaneC& lc, int lane) {
  if (rem < 32) {
#pragma unroll
    for (int i = 1; i < 4; i++)
      if (8 * i >= rem) *(u32x4*)(smem + stage_off + i * 1024 + lane * 16) = u32x4{0, 0, 0, 0};
  }
#pragma unroll
  for (int i = 0; i < 4; i++) {
    const u32 sv = (i & 1) ? (st[i >> 1] >> 16) : (st[i >> 1] & 0xffffu);
    const u32 voff = (sv << 7) | ((i & 1) ? lc.loff1 : lc.loff0);
    if (i == 0 || 8 * i < rem)
      __builtin_amdgcn_raw_ptr_buffer_load_lds(rs, (lds_vptr)(smem + stage_off + i * 1024), 16, voff, 0, 0, 0);
  }
}

__device__ __forceinline__ u32x4 build_s(u32x4 wa, u32x4 wb, u32 nsh) {
  u32 s[8];
#pragma unroll
  for (int j = 0; j < 4; j++) {
    s[j] = wa[j] & (u32)__builtin_amdgcn_sbfe((int)wa[j], nsh, 1);
    s[4 + j] = wb[j] & (u32)__builtin_amdgcn_sbfe((int)wb[j], nsh, 1);
  }
  u32x4 r;
#pragma unroll
  for (int j = 0; j < 4; j++) r[j] = __builtin_amdgcn_perm(s[2 * j + 1], s[2 * j], 0x05040100u);
  return r;
}

__device__ __forceinline__ void gather_accum(f32x4 (&acc)[4], char* smem, int list_off, int stage_off, int n,
                                             __amdgpu_buffer_rsrc_t rs, const LaneC& lc, int lane) {
  if (n <= 0) return;
  const char* lp = smem + list_off + LIST_HDR;
  u32x4 wa = *(const u32x4*)(lp + lc.q32);
  u32x4 wb = *(const u32x4*)(lp + lc.q32 + 16);
  u32x2 st = *(const u32x2*)(lp + lc.g8);
  issue_chunk(smem, stage_off, rs, st, n, lc, lane);
  u32x4 S = build_s(wa, wb, lc.nsh);
  for (int rem = n; rem > 0; rem -= 32) {
    asm volatile("s_waitcnt vmcnt(0)" ::: "memory");
    s16x4 a[4][2];
#pragma unroll
    for (int t = 0; t < 4; t++) {
      const u32 ad = lc.tr0 ^ (u32)(t << 5);
      a[t][0] = __builtin_amdgcn_ds_read_tr16_b64_v4i16((lds_s16x4ptr)(uintptr_t)ad);
      a[t][1] = __builtin_amdgcn_ds_read_tr16_b64_v4i16((lds_s16x4ptr)(uintptr_t)(ad + 512));
    }
    lp += CHUNKB;
    const bool more = rem > 32;
    if (more) {
      wa = *(const u32x4*)(lp + lc.q32);
      wb = *(const u32x4*)(lp + lc.q32 + 16);
      st = *(const u32x2*)(lp + lc.g8);
    }
    asm volatile("s_waitcnt lgkmcnt(0)" ::: "memory");
    __builtin_amdgcn_sched_barrier(0);
    if (more) issue_chunk(smem, stage_off, rs, st, rem - 32, lc, lane);
    __builtin_amdgcn_sched_barrier(0);
#pragma unroll
    for (int t = 0; t < 4; t++) {
      typedef short s16x8 __attribute__((ext_vector_type(8)));
      s16x8 af = __builtin_shufflevector(a[t][0], a[t][1], 0, 1, 2, 3, 4, 5, 6, 7);
      acc[t] = __builtin_amdgcn_mfma_f32_16x16x32_bf16(__builtin_bit_cast(bf16x8, af), __builtin_bit_cast(bf16x8, S),
                                                       acc[t], 0, 0, 0);
    }
    if (more) S = build_s(wa, wb, lc.nsh);
  }
}

template <int NS>
__device__ __forceinline__ int sort_lists(char* smem, const u32 (&lo)[NS], const u32 (&hi)[NS], u32 vmask, int wv, int lane) {
  u32* s_hist = (u32*)(smem + L_HIST);
  u32* s_base = (u32*)(smem + L_BASE);
  u32* s_flag = (u32*)(smem + L_FLAG);
  u32 rk[NS];
#pragma unroll
  for (int k = 0; k < NS; k++) {
    rk[k] = 0;
    if ((vmask >> k) & 1u) rk[k] = atomicAdd(&s_hist[hi[k] & 127u], 1u);
  }
  __syncthreads();
  u32 h = s_hist[wv * NPH + (lane & 7)];
  u32 inc = h;
#pragma unroll
  for (int o = 1; o < 8; o <<= 1) { u32 y = __shfl_up(inc, o, 8); if ((lane & 7) >= o) inc += y; }
  const int nw = __builtin_amdgcn_readlane((int)inc, 7);
  if (lane < 8) s_base[wv * NPH + lane] = inc - h;
  if (nw > LIST_CAP && lane == 0) s_flag[0] = 1u;
  __syncthreads();
#pragma unroll
  for (int k = 0; k < NS; k++) {
    if ((vmask >> k) & 1u) {
      const u32 bin = hi[k] & 127u;
      const u32 pos = s_base[bin] + rk[k];
      if (pos < (u32)LIST_CAP) {
        char* cp = smem + L_LISTS + (bin >> 3) * LIST_BYTES + LIST_HDR + (pos >> 5) * CHUNKB;
        const u32 e = pos & 31u;
        *(unsigned short*)(cp + ((e & 7u) << 3) + ((e >> 3) << 1)) = (unsigned short)lo[k];
        *(u32*)(cp + 64 + (e << 2)) = (lo[k] >> 16) | (hi[k] & 0xffff0000u);
      }
    }
  }
  {
    char* rp = smem + L_LISTS + wv * LIST_BYTES;
    const u32 pos = (u32)nw + (u32)lane;
    if (lane < 32 && pos < (u32)((nw + 31) & ~31) && pos < (u32)LIST_CAP) {
      char* cp = rp + LIST_HDR + (pos >> 5) * CHUNKB;
      const u32 e = pos & 31u;
      *(unsigned short*)(cp + ((e & 7u) << 3) + ((e >> 3) << 1)) = (unsigned short)0xffffu;
      *(u32*)(cp + 64 + (e << 2)) = 0u;
    }
    if (lane == 0) *(u32*)rp = (u32)nw;
  }
  __syncthreads();
  return nw;
}

__device__ __forceinline__ void slow_bucket(f32x4 (&acc)[4], char* smem, const u32x2* __restrict__ part,
                                         const u32* __restrict__ cellinfo, int CH, __amdgpu_buffer_rsrc_t rs,
                                         const LaneC& lc, int b, int tid, int wv, int lane) {
  u32* s_hist = (u32*)(smem + L_HIST);
  u32* s_pre = (u32*)(smem + L_PRE);
  u32* s_ci = (u32*)(smem + L_CI);
  __syncthreads();
  if (tid < NP) s_ci[tid] = cellinfo[b * NP + tid];
  __syncthreads();
  if (tid < 64) {
    u32 l0 = s_ci[tid * 4] >> 16, l1 = s_ci[tid * 4 + 1] >> 16, l2 = s_ci[tid * 4 + 2] >> 16, l3 = s_ci[tid * 4 + 3] >> 16;
    const u32 t = l0 + l1 + l2 + l3;
    u32 inc = t;
    for (int o = 1; o < 64; o <<= 1) { u32 y = __shfl_up(inc, o, 64); if (tid >= o) inc += y; }
    const u32 ex = inc - t;
    s_pre[tid * 4] = ex; s_pre[tid * 4 + 1] = ex + l0; s_pre[tid * 4 + 2] = ex + l0 + l1; s_pre[tid * 4 + 3] = ex + l0 + l1 + l2;
    if (tid == 63) s_pre[NP] = inc;
  }
  __syncthreads();
  const int T = (int)s_pre[NP];
  for (int r0 = 0; r0 < T; r0 += LIST_CAP) {
    if (tid < WPB * NPH) s_hist[tid] = 0;
    u32 lo[1] = {0}, hi[1] = {0};
    u32 vm = 0;
    const int f = r0 + tid;
    if (tid < LIST_CAP && f < T) {
      int a = 0, c = NP;
      while (c - a > 1) { const int m = (a + c) >> 1; if (s_pre[m] <= (u32)f) a = m; else c = m; }
      const u32x2 en = part[(size_t)a * CH + (s_ci[a] & 0xffffu) + ((u32)f - s_pre[a])];
      lo[0] = en[0]; hi[0] = en[1]; vm = 1;
    }
    __syncthreads();
    const int nw = sort_lists<1>(smem, lo, hi, vm, wv, lane);
    gather_accum(acc, smem, L_LISTS + wv * LIST_BYTES, L_STAGE + wv * 4096, nw, rs, lc, lane);
    __syncthreads();
  }
}

template <int PASS>
__global__ __launch_bounds__(TPB) void k_spmm(const unsigned short* __restrict__ x, unsigned short* __restrict__ t1,
                                              const u32x2* __restrict__ part, const u32* __restrict__ cellinfo,
                                              char* __restrict__ image, const unsigned short* __restrict__ wfrag,
                                              unsigned short* __restrict__ out, int N, int CH) {
  extern __shared__ __attribute__((aligned(16))) char smem[];
  const int tid = threadIdx.x, b = blockIdx.x, lane = tid & 63;
  const int wv = __builtin_amdgcn_readfirstlane(tid >> 6);
  const u32 rowbytes = (u32)N * 128u;
  const __amdgpu_buffer_rsrc_t rs = mk_rsrc(PASS == 1 ? (const void*)x : (const void*)t1, rowbytes);

  LaneC lc;
  {
    const u32 g = lane >> 3, c = lane & 7;
    const u32 x0 = (g >> 1) & 1u;
    lc.loff0 = (c ^ (x0 << 1)) << 4;
    lc.loff1 = (c ^ ((x0 | 2u) << 1)) << 4;
    const u32 q = lane >> 4, r = (lane >> 2) & 3, pp = lane & 3;
    const u32 xs = ((q & 1u) << 1) | ((r >> 1) & 1u);
    lc.tr0 = (u32)(uintptr_t)(lds_vptr)(smem + L_STAGE + wv * 4096) + q * 1024 + r * 128 + xs * 32 + pp * 8;
    lc.g8 = g << 3;
    lc.q32 = 64 + 32 * q;
    lc.nsh = 16 + (lane & 15);
  }
  const u32 v = (u32)(b * WPB + wv);
  const u32 nstart = (v * (u32)N + ((1u << VB_LOG) - 1)) >> VB_LOG;
  const u32 nend = ((v + 1) * (u32)N + ((1u << VB_LOG) - 1)) >> VB_LOG;
  const u32 node = nstart + (lane & 15);
  const bool nvalid = node < nend;
  const u32 rowoff = node * 128u + (u32)(lane >> 4) * 32u;

  f32x4 acc[4];
#pragma unroll
  for (int t = 0; t < 4; t++) acc[t] = f32x4{0.f, 0.f, 0.f, 0.f};

  char* imgw = image + ((size_t)b * WPB + wv) * LIST_BYTES;

  if constexpr (PASS == 1) {
    u32* s_hist = (u32*)(smem + L_HIST);
    u32* s_flag = (u32*)(smem + L_FLAG);
    const int grp = tid >> 4;
    const u32x4 ci = *(const u32x4*)(cellinfo + b * NP + grp * 4);
    if (tid < WPB * NPH) s_hist[tid] = 0;
    if (tid == 0) s_flag[0] = 0;
    const __amdgpu_buffer_rsrc_t rp = mk_rsrc(part, (u32)NP * (u32)CH * 8u);
    const u32 idx = (tid & 15) * 2;
    u32 lo[8], hi[8];
    u32 vm = 0;
    bool big = false;
#pragma unroll
    for (int k = 0; k < 4; k++) {
      const u32 st = ci[k] & 0xffffu, len = ci[k] >> 16;
      const u32 off = (idx < len) ? ((u32)(grp * 4 + k) * (u32)CH + st + idx) * 8u : 0x80000000u;
      const u32x4 en = __builtin_amdgcn_raw_buffer_load_b128(rp, off, 0, 0);
      lo[2 * k] = en[0]; hi[2 * k] = en[1]; lo[2 * k + 1] = en[2]; hi[2 * k + 1] = en[3];
      vm |= (idx < len ? 1u : 0u) << (2 * k);
      vm |= (idx + 1 < len ? 1u : 0u) << (2 * k + 1);
      big |= len > 32u;
    }
    __syncthreads();
    if (big) s_flag[0] = 1u;
    const int nw = sort_lists<8>(smem, lo, hi, vm, wv, lane);
    const bool slow = s_flag[0] != 0u;
    if (!slow) {
      const int nb16 = (LIST_HDR + ((nw + 31) >> 5) * CHUNKB) >> 4;
      const u32x4* sp = (const u32x4*)(smem + L_LISTS + wv * LIST_BYTES);
      if (lane < nb16) ((u32x4*)imgw)[lane] = sp[lane];
      if (lane + 64 < nb16) ((u32x4*)imgw)[lane + 64] = sp[lane + 64];
      gather_accum(acc, smem, L_LISTS + wv * LIST_BYTES, L_STAGE + wv * 4096, nw, rs, lc, lane);
    } else {
      if (lane == 0) *(u32*)imgw = 0xffffffffu;
      slow_bucket(acc, smem, part, cellinfo, CH, rs, lc, b, tid, wv, lane);
    }
    if (nvalid) {
      u32x4 o0 = {pk_bf16(acc[0][0], acc[0][1]), pk_bf16(acc[0][2], acc[0][3]), pk_bf16(acc[1][0], acc[1][1]), pk_bf16(acc[1][2], acc[1][3])};
      u32x4 o1 = {pk_bf16(acc[2][0], acc[2][1]), pk_bf16(acc[2][2], acc[2][3]), pk_bf16(acc[3][0], acc[3][1]), pk_bf16(acc[3][2], acc[3][3])};
      u32x4* tp = (u32x4*)((char*)t1 + rowoff);
      tp[0] = o0; tp[1] = o1;
    }
  } else {
    {
      const __amdgpu_buffer_rsrc_t ri = mk_rsrc(imgw, LIST_BYTES);
      __builtin_amdgcn_raw_ptr_buffer_load_lds(ri, (lds_vptr)(smem + L_LISTS + wv * LIST_BYTES), 16, lane * 16, 0, 0, 0);
      __builtin_amdgcn_raw_ptr_buffer_load_lds(ri, (lds_vptr)(smem + L_LISTS + wv * LIST_BYTES + 1024), 16, lane * 16 + 1024, 0, 0, 0);
      const __amdgpu_buffer_rsrc_t rw = mk_rsrc(wfrag, 24 * 1024);
      __builtin_amdgcn_raw_ptr_buffer_load_lds(rw, (lds_vptr)(smem + L_WFRAG + wv * 1024), 16, wv * 1024 + lane * 16, 0, 0, 0);
      if (wv < 8)
        __builtin_amdgcn_raw_ptr_buffer_load_lds(rw, (lds_vptr)(smem + L_WFRAG + (16 + wv) * 1024), 16, (16 + wv) * 1024 + lane * 16, 0, 0, 0);
    }
    const __amdgpu_buffer_rsrc_t rx = mk_rsrc(x, rowbytes);
    u32x4 xf[2], tf[2];
    xf[0] = __builtin_amdgcn_raw_buffer_load_b128(rx, rowoff, 0, 0);
    xf[1] = __builtin_amdgcn_raw_buffer_load_b128(rx, rowoff + 16, 0, 0);
    tf[0] = __builtin_amdgcn_raw_buffer_load_b128(rs, rowoff, 0, 0);
    tf[1] = __builtin_amdgcn_raw_buffer_load_b128(rs, rowoff + 16, 0, 0);
    asm volatile("s_waitcnt vmcnt(0)" ::: "memory");
    const u32 hdr = *(const u32*)(smem + L_LISTS + wv * LIST_BYTES);
    const int nw = __builtin_amdgcn_readfirstlane((int)hdr);
    if (nw >= 0) {
      gather_accum(acc, smem, L_LISTS + wv * LIST_BYTES, L_STAGE + wv * 4096, nw, rs, lc, lane);
    } else {
      slow_bucket(acc, smem, part, cellinfo, CH, rs, lc, b, tid, wv, lane);
    }
    __syncthreads();
    u32x4 t2[2];
#pragma unroll
    for (int s = 0; s < 2; s++) {
      t2[s][0] = pk_bf16(2.f * acc[2 * s][0] - bf_lo(xf[s][0]), 2.f * acc[2 * s][1] - bf_hi(xf[s][0]));
      t2[s][1] = pk_bf16(2.f * acc[2 * s][2] - bf_lo(xf[s][1]), 2.f * acc[2 * s][3] - bf_hi(xf[s][1]));
      t2[s][2] = pk_bf16(2.f * acc[2 * s + 1][0] - bf_lo(xf[s][2]), 2.f * acc[2 * s + 1][1] - bf_hi(xf[s][2]));
      t2[s][3] = pk_bf16(2.f * acc[2 * s + 1][2] - bf_lo(xf[s][3]), 2.f * acc[2 * s + 1][3] - bf_hi(xf[s][3]));
    }
    f32x4 o[4];
#pragma unroll
    for (int u = 0; u < 4; u++) o[u] = f32x4{0.f, 0.f, 0.f, 0.f};
    const char* wl = smem + L_WFRAG + lane * 16;
#pragma unroll
    for (int k = 0; k < 3; k++) {
#pragma unroll
      for (int s = 0; s < 2; s++) {
        const u32x4 bf = (k == 0) ? xf[s] : (k == 1) ? tf[s] : t2[s];
#pragma unroll
        for (int u = 0; u < 4; u++) {
          const u32x4 af = *(const u32x4*)(wl + ((k * 2 + s) * 4 + u) * 1024);
          o[u] = __builtin_amdgcn_mfma_f32_16x16x32_bf16(__builtin_bit_cast(bf16x8, af), __builtin_bit_cast(bf16x8, bf), o[u], 0, 0, 0);
        }
      }
    }
    if (nvalid) {
      u32x4 o0 = {pk_bf16(o[0][0], o[0][1]), pk_bf16(o[0][2], o[0][3]), pk_bf16(o[1][0], o[1][1]), pk_bf16(o[1][2], o[1][3])};
      u32x4 o1 = {pk_bf16(o[2][0], o[2][1]), pk_bf16(o[2][2], o[2][3]), pk_bf16(o[3][0], o[3][1]), pk_bf16(o[3][2], o[3][3])};
      u32x4* op = (u32x4*)((char*)out + rowoff);
      op[0] = o0; op[1] = o1;
    }
  }
}

extern "C" void kernel_launch(void* const* d_in, const int* in_sizes, int n_in, void* d_out, int out_size, void* d_ws,
                              size_t ws_size, hipStream_t stream) {
  const unsigned short* x = (const unsigned short*)d_in[0];
  const int* src = (const int*)d_in[1];
  const int* dst = (const int*)d_in[2];
  const unsigned short* ew = (const unsigned short*)d_in[3];
  const unsigned short* w = (const unsigned short*)d_in[4];
  const int N = in_sizes[0] / 64;
  const int E = in_sizes[1];
  int CH = (E + NP - 1) / NP;
  CH = (CH + 3) & ~3;
  const u32 magic = (u32)((((unsigned long long)1 << 44) + (unsigned long long)N - 1) / (unsigned long long)N);

  char* ws = (char*)d_ws;
  size_t off = 0;
  u32x2* part = (u32x2*)(ws + off); off += ((size_t)NP * CH * 8 + 255) & ~(size_t)255;
  u32* cellinfo = (u32*)(ws + off); off += (size_t)NB * NP * 4;
  char* image = ws + off; off += (size_t)NB * WPB * LIST_BYTES;
  unsigned short* t1 = (unsigned short*)(ws + off); off += ((size_t)N * 128 + 255) & ~(size_t)255;
  unsigned short* wfrag = (unsigned short*)(ws + off); off += 24 * 1024;

  (void)hipFuncSetAttribute((const void*)k_spmm<1>, hipFuncAttributeMaxDynamicSharedMemorySize, L_TOTAL1);
  (void)hipFuncSetAttribute((const void*)k_spmm<2>, hipFuncAttributeMaxDynamicSharedMemorySize, L_TOTAL2);
  k_part<<<NP, TPB, 0, stream>>>(src, dst, ew, w, part, cellinfo, wfrag, N, E, CH, magic);
  k_spmm<1><<<NB, TPB, L_TOTAL1, stream>>>(x, t1, part, cellinfo, image, wfrag, (unsigned short*)d_out, N, CH);
  k_spmm<2><<<NB, TPB, L_TOTAL2, stream>>>(x, t1, part, cellinfo, image, wfrag, (unsigned short*)d_out, N, CH);
}
```

```cpp
#include <hip/hip_runtime.h>
#include <hip/hip_bf16.h>
#include <stdint.h>
#include <type_traits>

typedef unsigned u32;
typedef u32 u32x4 __attribute__((ext_vector_type(4)));
typedef u32 u32x2 __attribute__((ext_vector_type(2)));
typedef float f32x4 __attribute__((ext_vector_type(4)));
typedef float f32x2 __attribute__((ext_vector_type(2)));
typedef __bf16 bf16x8 __attribute__((ext_vector_type(8)));
typedef __bf16 bf16x2 __attribute__((ext_vector_type(2)));
typedef short s16x4 __attribute__((ext_vector_type(4)));
typedef __attribute__((address_space(3))) s16x4* lds_s16x4ptr;

constexpr int NB = 256;
constexpr int NP = 256;
constexpr int WPB = 16;
constexpr int TPB = WPB * 64;
constexpr int NPH = 8;
constexpr int MAXCH = 4096;
constexpr int MAXT3 = 1536;
constexpr int RND = 1536;
constexpr int LCAP = 320;
constexpr int KW = 8;
constexpr int PT = KW * 64;
constexpr int PG = 16 / KW;
constexpr int RU = 2;
constexpr int PF = 4;
constexpr int BAR_S = 16;
constexpr int BAR_R = 8;
constexpr int BAR_STR = 64;
constexpr u32 OOB = 0x80000000u;
constexpr int SC1 = 16;

constexpr int L_B = 128;
constexpr int L_A = 5376;
constexpr int L_MISC = L_A + WPB * LCAP * 4;
constexpr int L_DUMP = L_MISC + 256;
constexpr int L_RING = 26624;
constexpr int L_MAP = L_RING;
constexpr int L_CNT = L_RING + RND * 4;
constexpr int L_WFRAG = L_RING + WPB * RU * 2048;
constexpr int L_TOTAL = L_WFRAG + 24 * 1024;
static_assert(L_B + WPB * LCAP <= L_A && L_RING % 2048 == 0 && L_DUMP + 256 <= L_RING, "LDS map");
static_assert(L_TOTAL <= 163840 && L_TOTAL > 81920, "exactly one workgroup per CU");

__device__ __forceinline__ __amdgpu_buffer_rsrc_t mk_rsrc(const void* p, u32 bytes) {
  return __builtin_amdgcn_make_buffer_rsrc((void*)p, 0, bytes, 0x00020000);
}
__device__ __forceinline__ u32x4 mk_srd(const void* p, u32 bytes) {
  const unsigned long long a = (unsigned long long)p;
  u32x4 r;
  r[0] = __builtin_amdgcn_readfirstlane((u32)a);
  r[1] = __builtin_amdgcn_readfirstlane((u32)(a >> 32) & 0xffffu);
  r[2] = __builtin_amdgcn_readfirstlane(bytes);
  r[3] = 0x00020000u;
  return r;
}
__device__ __forceinline__ void dma16(u32x4 srd, u32 ldsaddr, u32 voff) {
  u32 keep;
  asm volatile("s_mov_b32 %0, m0\n\ts_mov_b32 m0, %1\n\ts_nop 0\n\tbuffer_load_dwordx4 %2, %3, 0 offen lds\n\ts_mov_b32 m0, %0"
               : "=&s"(keep) : "s"(ldsaddr), "v"(voff), "s"(srd) : "memory");
}
template <bool DEV>
__device__ __forceinline__ void dma16x2(u32x4 srd, u32 ldsaddr, u32 voff0, u32 voff1) {
  u32 keep;
  if constexpr (DEV)
    asm volatile("s_mov_b32 %0, m0\n\ts_mov_b32 m0, %1\n\ts_nop 0\n\tbuffer_load_dwordx4 %2, %4, 0 offen sc1 lds\n\t"
                 "s_add_u32 m0, %1, 0x400\n\ts_nop 0\n\tbuffer_load_dwordx4 %3, %4, 0 offen sc1 lds\n\ts_mov_b32 m0, %0"
                 : "=&s"(keep) : "s"(ldsaddr), "v"(voff0), "v"(voff1), "s"(srd) : "memory", "scc");
  else
    asm volatile("s_mov_b32 %0, m0\n\ts_mov_b32 m0, %1\n\ts_nop 0\n\tbuffer_load_dwordx4 %2, %4, 0 offen lds\n\t"
                 "s_add_u32 m0, %1, 0x400\n\ts_nop 0\n\tbuffer_load_dwordx4 %3, %4, 0 offen lds\n\ts_mov_b32 m0, %0"
                 : "=&s"(keep) : "s"(ldsaddr), "v"(voff0), "v"(voff1), "s"(srd) : "memory", "scc");
}
__device__ __forceinline__ void dma4(u32x4 srd, u32 ldsaddr, u32 voff) {
  u32 keep;
  asm volatile("s_mov_b32 %0, m0\n\ts_mov_b32 m0, %1\n\ts_nop 0\n\tbuffer_load_dword %2, %3, 0 offen sc1 lds\n\ts_mov_b32 m0, %0"
               : "=&s"(keep) : "s"(ldsaddr), "v"(voff), "s"(srd) : "memory");
}
__device__ __forceinline__ u32x4 ld16_asm(u32x4 srd, u32 voff) {
  u32x4 r;
  asm volatile("buffer_load_dwordx4 %0, %1, %2, 0 offen" : "=v"(r) : "v"(voff), "s"(srd) : "memory");
  return r;
}
#define WAIT_VM(n) asm volatile("s_waitcnt vmcnt(" #n ")" ::: "memory")
template <int N_> __device__ __forceinline__ void wait_vm() { asm volatile("s_waitcnt vmcnt(%0)" :: "n"(N_) : "memory"); }

__device__ __forceinline__ u32 pk_bf16(float a, float b) {
  f32x2 v = {a, b};
  return __builtin_bit_cast(u32, __builtin_convertvector(v, bf16x2));
}
__device__ __forceinline__ float bf_lo(u32 v) { return __uint_as_float(v << 16); }
__device__ __forceinline__ float bf_hi(u32 v) { return __uint_as_float(v & 0xffff0000u); }

__device__ __forceinline__ u32 wave_scan_incl(u32 v) {
  asm volatile("s_nop 1\n\tv_add_u32_dpp %0, %0, %0 row_shr:1 row_mask:0xf bank_mask:0xf bound_ctrl:0\n\t"
      "s_nop 1\n\tv_add_u32_dpp %0, %0, %0 row_shr:2 row_mask:0xf bank_mask:0xf bound_ctrl:0\n\t"
      "s_nop 1\n\tv_add_u32_dpp %0, %0, %0 row_shr:4 row_mask:0xf bank_mask:0xf bound_ctrl:0\n\t"
      "s_nop 1\n\tv_add_u32_dpp %0, %0, %0 row_shr:8 row_mask:0xf bank_mask:0xf bound_ctrl:0\n\t"
      "s_nop 1\n\tv_add_u32_dpp %0, %0, %0 row_bcast:15 row_mask:0xa bank_mask:0xf\n\t"
      "s_nop 1\n\tv_add_u32_dpp %0, %0, %0 row_bcast:31 row_mask:0xc bank_mask:0xf\n\ts_nop 1"
      : "+v"(v));
  return v;
}
__device__ __forceinline__ u32 mulhi24(u32 a, u32 b) {
  u32 r; asm("v_mul_hi_u32_u24 %0, %1, %2" : "=v"(r) : "s"(b), "v"(a)); return r;
}
__device__ __forceinline__ u32 madi24(u32 a, int b, u32 c) {
  u32 r; asm("v_mad_i32_i24 %0, %1, %2, %3" : "=v"(r) : "v"(a), "s"(b), "v"(c)); return r;
}
__device__ __forceinline__ u32 div3(u32 r) { return __umul24(r, 0xAAABu) >> 17; }

template <bool PASS2, bool XROWS, class PRE>
__device__ __forceinline__ void gather_accum(f32x4 (&acc)[4], char* smem, int n, u32x4 srd, int wv, int lane,
                                             u32 vr0, u32 vr1, PRE&& before_first_load) {
  constexpr int DIR = PASS2 ? -1 : 1;
  const int nu = (n + 15) >> 4;
  const u32 g = lane >> 3, c = lane & 7, q = lane >> 4;
  const u32 loff = (c ^ (g & 6u)) << 4;
  const u32 ring0 = (u32)(L_RING + wv * RU * 2048);
  const u32 tr0 = ring0 + ((u32)(lane & 60) << 5) + ((u32)(lane & 24) << 2) + (u32)(lane & 3) * 8;
  const u32 tra[4] = {tr0, tr0 ^ 32u, tr0 ^ 64u, tr0 ^ 96u};
  const u32 nloc = (lane & 15) * 16 + wv;
  constexpr int BIAS = PASS2 ? (2 * RU - 1) : 0;
  const int first = wv * LCAP + (PASS2 ? (nu - 1) * 16 : 0) - BIAS * 16;
  const char* sA = smem + L_A + first * 4 + g * 4;
  const char* eA = smem + L_A + first * 4 + q * 16;
  const char* eB = smem + L_B + first + q * 4;
  {
    u32 s0[RU], s1[RU];
#pragma unroll
    for (int u = 0; u < RU; u++) {
      s0[u] = (*(const unsigned short*)(sA + (BIAS + DIR * u) * 64) << 7) | loff;
      s1[u] = (*(const unsigned short*)(sA + (BIAS + DIR * u) * 64 + 32) << 7) | loff;
    }
    if (PASS2) asm volatile("" : "+v"(s0[0]), "+v"(s1[0]), "+v"(s0[1]), "+v"(s1[1]));
    before_first_load();
    __builtin_amdgcn_s_setprio(3);
#pragma unroll
    for (int u = 0; u < RU; u++)
      if (u < nu) dma16x2<PASS2>(srd, ring0 + u * 2048, s0[u], s1[u]);
      else if (XROWS && u == nu) dma16x2<false>(srd, ring0 + u * 2048, vr0, vr1);
  }
  int u = 0;
  auto step = [&](auto SLOT) {
    constexpr int S_ = decltype(SLOT)::value;
    {
      const int xr = 2 * (nu - u - RU + (wv >> 2));
      if (xr < 8) __builtin_amdgcn_s_setprio(0);
      else if (xr < 15) __builtin_amdgcn_s_setprio(1);
      else if (xr < 22) __builtin_amdgcn_s_setprio(2);
    }
    const u32x4 ea = *(const u32x4*)(eA + (BIAS + DIR * S_) * 64);
    const u32 eb = *(const u32*)(eB + (BIAS + DIR * S_) * 16);
    const u32 s0 = *(const unsigned short*)(sA + (BIAS + DIR * (S_ + RU)) * 64);
    const u32 s1 = *(const unsigned short*)(sA + (BIAS + DIR * (S_ + RU)) * 64 + 32);
    __builtin_amdgcn_sched_barrier(0);
    if (XROWS || u + 1 < nu) WAIT_VM(2); else WAIT_VM(0);
    s16x4 a[4];
#pragma unroll
    for (int t = 0; t < 4; t++) a[t] = __builtin_amdgcn_ds_read_tr16_b64_v4i16((lds_s16x4ptr)(uintptr_t)(tra[t] + S_ * 2048));
    __builtin_amdgcn_sched_barrier(0);
    u32 sv[4];
#pragma unroll
    for (int j = 0; j < 4; j++) sv[j] = (((eb >> (8 * j)) & 0xffu) == nloc) ? ea[j] : 0u;
    u32x2 S;
    S[0] = __builtin_amdgcn_perm(sv[1], sv[0], 0x07060302u);
    S[1] = __builtin_amdgcn_perm(sv[3], sv[2], 0x07060302u);
    u32 v0 = (s0 << 7) | loff, v1 = (s1 << 7) | loff;
    if (XROWS && u + RU == nu) { v0 = vr0; v1 = vr1; }
    asm volatile("s_waitcnt lgkmcnt(0)" : "+v"(a[0]), "+v"(a[1]), "+v"(a[2]), "+v"(a[3]), "+v"(S), "+v"(v0), "+v"(v1) :: "memory");
    if (u + RU < nu + (XROWS ? 1 : 0)) dma16x2<PASS2>(srd, ring0 + S_ * 2048, v0, v1);
    __builtin_amdgcn_sched_barrier(0);
#pragma unroll
    for (int t = 0; t < 4; t++)
      acc[t] = __builtin_amdgcn_mfma_f32_16x16x16bf16_1k(a[t], __builtin_bit_cast(s16x4, S), acc[t], 0, 0, 0);
    u++;
  };
#pragma clang loop unroll(disable)
  while (u < nu) {
    step(std::integral_constant<int, 0>{});
    if (u >= nu) break;
    step(std::integral_constant<int, 1>{});
    sA += DIR * RU * 64; eA += DIR * RU * 64; eB += DIR * RU * 16;
  }
  __builtin_amdgcn_s_setprio(0);
}

struct Ctx {
  u32x4 sp, sx, st;
  u32x4 ci;
  int b, tid, wv, lane;
  u32 N, npb, phm;
};

template <bool FIRST>
__device__ __forceinline__ bool build_lists(char* smem, Ctx& cx, int r0, int& R, int& T3, int& nw) {
  const int tid = cx.tid, wv = cx.wv, lane = cx.lane;
  u32* s_map = (u32*)(smem + L_MAP);
  char* s_cnt = smem + L_CNT + wv * 4;
  const u32 rot = 7u - (u32)(cx.b >> 5);
  char* lA = smem + L_A;
  char* lB = smem + L_B;
  if (!FIRST || wv < 8) WAIT_VM(0);
  asm volatile("" : "+v"(cx.ci) :: "memory");
  if (wv < 8) {
    const u32x4 ci = cx.ci;
    u32 len[4], ntr[4];
#pragma unroll
    for (int k = 0; k < 4; k++) { len[k] = ci[k] >> 19; ntr[k] = div3(len[k] + 2); }
    const u32 tsum = ntr[0] + ntr[1] + ntr[2] + ntr[3];
    const u32 tinc = wave_scan_incl(tsum);
    if (tid == 63) *(u32*)(smem + L_MISC) = tinc;
    int pre = (int)(tinc - tsum) - r0;
    if (FIRST && __builtin_expect(__builtin_amdgcn_readlane((int)tinc, 63) <= R, 1)) {
      const u32 mx = max(max(ntr[0], ntr[1]), max(ntr[2], ntr[3]));
      u32 j = wv;
#pragma clang loop unroll(disable)
      do {
        u32 pk = pre + j;
#pragma unroll
        for (int k = 0; k < 4; k++) {
          if (j < ntr[k]) s_map[pk] = ((ci[k] & 0x7ffffu) + j) | (min(3u, len[k] - 3u * j) << 30);
          pk += ntr[k];
        }
        j += 8;
      } while (__any(j < mx));
    } else {
#pragma unroll
      for (int k = 0; k < 4; k++) {
        const int jh = min((int)ntr[k], R - pre);
#pragma clang loop unroll(disable) vectorize(disable)
        for (int j = max(0, -pre) + wv; j < jh; j += 8)
          s_map[pre + j] = ((ci[k] & 0x7ffffu) + j) | (min(3u, len[k] - 3u * j) << 30);
        pre += ntr[k];
      }
    }
  } else {
    *(u32x4*)(smem + L_CNT + (tid - 512) * 16) = u32x4{0, 0, 0, 0};
    if (tid < 512 + 32) *(u32x4*)(smem + L_CNT + tid * 16) = u32x4{0, 0, 0, 0};
    if (tid == 512) {
      *(u32*)(smem + L_MISC + 8) = 0;
      if (FIRST) *(u32*)(smem + L_MISC + 12) = 0;
    }
  }
  __syncthreads();
  u32 t3v = *(const u32*)(smem + L_MISC), m0 = s_map[tid], m1 = s_map[tid + TPB];
  asm volatile("" : "+v"(t3v), "+v"(m0), "+v"(m1));
  T3 = __builtin_amdgcn_readfirstlane(t3v);
  const int nf = min(T3 - r0, R);
  const bool two = wv * 64 + TPB < nf;
  m0 = (tid < nf) ? m0 : (OOB >> 4);
  m1 = (tid + TPB < nf) ? m1 : (OOB >> 4);
  u32x4 e0 = ld16_asm(cx.sp, m0 << 4);
  u32x4 e1;
  if (two) e1 = ld16_asm(cx.sp, m1 << 4);
  if (PF > 0 && FIRST) {
    __builtin_amdgcn_s_barrier();
    if (wv >= WPB - PF) {
      u32 l = (((u32)(cx.b >> 5) + 1u) & 7u) * 32u * cx.npb + (u32)(TPB - 1 - tid) * 32u + (u32)(cx.b & 31);
      l = l >= cx.N ? l - cx.N : l;
      dma4(cx.sx, L_DUMP, l << 7);
      WAIT_VM(1);
    } else {
      WAIT_VM(0);
    }
  } else {
    WAIT_VM(0);
  }
  asm volatile("" : "+v"(e0), "+v"(e1) :: "memory");
  auto cadr = [&](u32 lo, u32 lb) {
    return __umul24(((lb & 15u) << 3) | (((__umul24(lo & 0xffffu, cx.phm) >> 24) + rot) & 7u), 68u);
  };
  const u32 ca0 = cadr(e0[0], e0[3]), ca1 = cadr(e0[1], e0[3] >> 8), ca2 = cadr(e0[2], e0[3] >> 16);
  u32 rk0 = 0, rk1 = 0, rk2 = 0, rk3 = 0, rk4 = 0, rk5 = 0, ca3 = 0, ca4 = 0, ca5 = 0;
  const u32 v0 = m0 >> 30, v1 = m1 >> 30;
  if (v0 > 0) rk0 = atomicAdd((u32*)(s_cnt + ca0), 1u);
  asm volatile("" ::: "memory");
  if (v0 > 1) rk1 = atomicAdd((u32*)(s_cnt + ca1), 1u);
  asm volatile("" ::: "memory");
  if (v0 > 2) rk2 = atomicAdd((u32*)(s_cnt + ca2), 1u);
  if (two) {
    ca3 = cadr(e1[0], e1[3]); ca4 = cadr(e1[1], e1[3] >> 8); ca5 = cadr(e1[2], e1[3] >> 16);
    if (v1 > 0) rk3 = atomicAdd((u32*)(s_cnt + ca3), 1u);
    asm volatile("" ::: "memory");
    if (v1 > 1) rk4 = atomicAdd((u32*)(s_cnt + ca4), 1u);
    asm volatile("" ::: "memory");
    if (v1 > 2) rk5 = atomicAdd((u32*)(s_cnt + ca5), 1u);
  }
  __syncthreads();
  {
    const int ws = wv * LCAP;
    u32* cp = (u32*)(smem + L_CNT) + (wv * NPH + (lane >> 3)) * 17 + (lane & 7) * 2;
    const u32 c0 = cp[0], c1 = cp[1];
    const u32 t = c0 + c1;
    const u32 inc = wave_scan_incl(t);
    const u32 ex = inc - t + ws;
    cp[0] = ex; cp[1] = ex + c0;
    nw = __builtin_amdgcn_readlane((int)inc, 63);
    if (nw > LCAP) {
      if (lane == 0) *(u32*)(smem + L_MISC + 8) = 1;
    } else if (nw + lane < ((nw + 15) & ~15)) {
      *(u32*)(lA + (ws + nw + lane) * 4) = 0x0000ffffu;
      *(unsigned char*)(lB + ws + nw + lane) = 0xffu;
    }
  }
  __syncthreads();
  u32 p0 = *(const u32*)(s_cnt + ca0), p1 = *(const u32*)(s_cnt + ca1), p2 = *(const u32*)(s_cnt + ca2);
  u32 p3 = 0, p4 = 0, p5 = 0;
  if (two) { p3 = *(const u32*)(s_cnt + ca3); p4 = *(const u32*)(s_cnt + ca4); p5 = *(const u32*)(s_cnt + ca5); }
  u32 ovf = *(const u32*)(smem + L_MISC + 8);
  asm volatile("" : "+v"(p0), "+v"(p1), "+v"(p2), "+v"(p3), "+v"(p4), "+v"(p5), "+v"(ovf));
  if (__builtin_expect(__builtin_amdgcn_readfirstlane(ovf) != 0, 0)) {
    R = max(96, min(R, nf) >> 1);
    __syncthreads();
    return false;
  }
  auto put = [&](u32 pos, u32 lo, u32 lb) {
    *(u32*)(lA + pos * 4) = lo;
    *(unsigned char*)(lB + pos) = (unsigned char)lb;
  };
  if (v0 > 0) put(p0 + rk0, e0[0], e0[3]);
  if (v0 > 1) put(p1 + rk1, e0[1], e0[3] >> 8);
  if (v0 > 2) put(p2 + rk2, e0[2], e0[3] >> 16);
  if (two) {
    if (v1 > 0) put(p3 + rk3, e1[0], e1[3]);
    if (v1 > 1) put(p4 + rk4, e1[1], e1[3] >> 8);
    if (v1 > 2) put(p5 + rk5, e1[2], e1[3] >> 16);
  }
  __syncthreads();
  return true;
}

template <bool PASS2>
__device__ __forceinline__ void slow_rounds(f32x4 (&acc)[4], char* smem, Ctx& cx, int R) {
  int r0 = 0, T3 = 1, nw = 0;
#pragma clang loop unroll(disable)
  while (r0 < T3) {
    __syncthreads();
    if (!build_lists<false>(smem, cx, r0, R, T3, nw)) continue;
    gather_accum<PASS2, false>(acc, smem, nw, PASS2 ? cx.st : cx.sx, cx.wv, cx.lane, 0, 0, [] {});
    r0 += R;
  }
}

__device__ __forceinline__ void dense_part(f32x4 (&o)[4], const char* wl, int k, const u32x4 (&bf)[2]) {
#pragma unroll
  for (int s = 0; s < 2; s++) {
#pragma unroll
    for (int u = 0; u < 4; u++) {
      const u32x4 af = *(const u32x4*)(wl + ((k * 2 + s) * 4 + u) * 1024);
      o[u] = __builtin_amdgcn_mfma_f32_16x16x32_bf16(__builtin_bit_cast(bf16x8, af), __builtin_bit_cast(bf16x8, bf[s]), o[u], 0, 0, 0);
    }
  }
}
__device__ __forceinline__ void pack_rows(const f32x4 (&a)[4], u32x4 (&o)[2]) {
  o[0] = u32x4{pk_bf16(a[0][0], a[0][1]), pk_bf16(a[0][2], a[0][3]), pk_bf16(a[1][0], a[1][1]), pk_bf16(a[1][2], a[1][3])};
  o[1] = u32x4{pk_bf16(a[2][0], a[2][1]), pk_bf16(a[2][2], a[2][3]), pk_bf16(a[3][0], a[3][1]), pk_bf16(a[3][2], a[3][3])};
}
template <int AUX>
__device__ __forceinline__ void store_rows(char* slot, u32 fo, const u32x4 (&o)[2], __amdgpu_buffer_rsrc_t r, u32 vr0, u32 vr1,
                                           int lane) {
  *(u32x4*)(slot + fo) = o[0];
  *(u32x4*)(slot + fo + 16) = o[1];
  const u32x4 r0 = *(const u32x4*)(slot + lane * 16);
  const u32x4 r1 = *(const u32x4*)(slot + 1024 + lane * 16);
  __builtin_amdgcn_raw_buffer_store_b128(r0, r, vr0, 0, AUX);
  __builtin_amdgcn_raw_buffer_store_b128(r1, r, vr1, 0, AUX);
}

extern "C" __global__ __launch_bounds__(PT) void k_part(const int* __restrict__ src, const int* __restrict__ dst,
                                                        const unsigned short* __restrict__ ew, const unsigned short* __restrict__ w,
                                                        u32x4* __restrict__ part, u32* __restrict__ cellinfo,
                                                        unsigned short* __restrict__ wfrag, u32* __restrict__ bar,
                                                        int E, int CH, int CH3, u32 npb, u32 magic) {
  __shared__ __attribute__((aligned(16))) u32 s_tab[(KW + 1) * NB];
  __shared__ __attribute__((aligned(16))) u32x2 s_e[MAXT3 * 3];
  __shared__ u32 s_ntot;
  const int tid = threadIdx.x, p = blockIdx.x, lane = tid & 63;
  const int wid = __builtin_amdgcn_readfirstlane(tid >> 6);
  const int e0 = p * CH;
  int n = E - e0; n = n < 0 ? 0 : (n > CH ? CH : n);
  u32x4 d4[PG], s4[PG];
  u32x2 w4[PG];
  u32 wval = 0, cpf = 0;
  {
    u32 eo[PG];
#pragma unroll
    for (int g = 0; g < PG; g++) eo[g] = (g * PT * 4 + tid * 4 < n) ? (u32)(e0 + g * PT * 4 + tid * 4) * 2u : 0x40000000u;
    const u32x4 sd = mk_srd(dst, (u32)E * 4u), ss = mk_srd(src, (u32)E * 4u), sw = mk_srd(ew, ((u32)E * 2u + 3u) & ~3u);
#pragma unroll
    for (int g = 0; g < PG; g++) d4[g] = ld16_asm(sd, eo[g] * 2u);
    {
      unsigned long long pc; u32 off, own, len;
      asm volatile("s_getpc_b64 %0\n.Lcpf_mark:\n\ts_mov_b32 %1, .Lcpf_mark-k_part\n\ts_mov_b32 %2, k_cheb-k_part\n\ts_mov_b32 %3, k_text_end-k_part"
                   : "=s"(pc), "=s"(off), "=s"(own), "=s"(len));
      len = len < 0x40000u ? len : 0u;
      const u32 o = tid < 32 ? tid * 128u : own + ((u32)(p >> 3) * 6u + tid - 32u) * 128u;
      asm volatile("buffer_load_dword %0, %1, %2, 0 offen" : "=v"(cpf) : "v"(tid < 38 ? o : OOB), "s"(mk_srd((const void*)(pc - off), len)) : "memory");
    }
#pragma unroll
    for (int g = 0; g < PG; g++) {
      s4[g] = ld16_asm(ss, eo[g] * 2u);
      asm volatile("buffer_load_dwordx2 %0, %1, %2, 0 offen" : "=v"(w4[g]) : "v"(eo[g]), "s"(sw) : "memory");
    }
    const int o = p * PT + tid;
    const int j = o & 7, l = (o >> 3) & 63, u = (o >> 9) & 3, sx = (o >> 11) & 1, k = o >> 12;
    const int i = l & 15, q = l >> 4;
    const int e = 16 * (i >> 2) + 4 * u + (i & 3);
    const int dd = (k == 1) ? (16 * (2 * sx + (j >> 2)) + 4 * q + (j & 3)) : (16 * q + 8 * sx + j);
    asm volatile("buffer_load_ushort %0, %1, %2, 0 offen" : "=v"(wval) : "v"((k * 4096 + dd * 64 + e) * 2), "s"(mk_srd(w, 3 * 4096 * 2)) : "memory");
  }
  u32* cnt = s_tab + wid * NB;
  *(u32x4*)(cnt + lane * 4) = u32x4{0, 0, 0, 0};
  u32 ha[PG][4], rk[PG][4], lb[PG][4];
#pragma unroll
  for (int g = 0; g < PG; g++) {
    if (g == 0) wait_vm<3 * PG + 1>(); else if (g == 1) wait_vm<3 * PG>(); else if (g == 2) wait_vm<3 * PG - 1>(); else wait_vm<3 * PG - 2>();
    asm volatile("" : "+v"(d4[g]) :: "memory");
#pragma unroll
    for (int k = 0; k < 4; k++) ha[g][k] = mulhi24(d4[g][k] << 8, magic);
    if (g * PT * 4 + tid * 4 < n) {
#pragma unroll
      for (int k = 0; k < 4; k++) rk[g][k] = atomicAdd(&cnt[ha[g][k]], 1u);
#pragma unroll
      for (int k = 0; k < 4; k++) lb[g][k] = madi24(ha[g][k], -(int)npb, d4[g][k]);
    }
  }
  __syncthreads();
  if (wid == KW - 1) {
    u32x4 c[KW];
#pragma unroll
    for (int r = 0; r < KW; r++) c[r] = *(const u32x4*)(s_tab + r * NB + lane * 4);
    u32x4 h = c[0];
#pragma unroll
    for (int r = 1; r < KW; r++) h += c[r];
    const u32 t0 = div3(h[0] + 2), t1 = div3(h[1] + 2), t2 = div3(h[2] + 2), t3 = div3(h[3] + 2);
    const u32 t = t0 + t1 + t2 + t3;
    const u32 inc = wave_scan_incl(t);
    const u32 ex = inc - t;
    const u32x4 bs = {ex, ex + t0, ex + t0 + t1, ex + t0 + t1 + t2};
    u32x4 run = bs * 3u;
#pragma unroll
    for (int r = 0; r < KW; r++) { *(u32x4*)(s_tab + r * NB + lane * 4) = run; run += c[r]; }
    *(u32x4*)(s_tab + KW * NB + lane * 4) = (bs + (u32)p * (u32)CH3) | (h << 19);
    if (lane == 63) s_ntot = inc;
  }
  __syncthreads();
  u32 xo[PG][4];
#pragma unroll
  for (int g = 0; g < PG; g++) {
#pragma unroll
    for (int k = 0; k < 4; k++) xo[g][k] = cnt[ha[g][k]];
  }
  WAIT_VM(0);
#pragma unroll
  for (int g = 0; g < PG; g++) asm volatile("" : "+v"(s4[g]), "+v"(w4[g]) :: "memory");
  asm volatile("" : "+v"(wval), "+v"(cpf) :: "memory");
#pragma unroll
  for (int g = 0; g < PG; g++) {
    const int i0 = g * PT * 4 + tid * 4;
    if (i0 < n) {
      if (i0 + 3 >= n) {
#pragma unroll
        for (int k = 1; k < 4; k++)
          if (i0 + k >= n) { s4[g][k] = 0xffffu; w4[g][k >> 1] &= (k & 1) ? 0x0000ffffu : 0u; }
      }
      s_e[rk[g][0] + xo[g][0]] = u32x2{__builtin_amdgcn_perm(w4[g][0], s4[g][0], 0x05040100u), lb[g][0]};
      s_e[rk[g][1] + xo[g][1]] = u32x2{__builtin_amdgcn_perm(w4[g][0], s4[g][1], 0x07060100u), lb[g][1]};
      s_e[rk[g][2] + xo[g][2]] = u32x2{__builtin_amdgcn_perm(w4[g][1], s4[g][2], 0x05040100u), lb[g][2]};
      s_e[rk[g][3] + xo[g][3]] = u32x2{__builtin_amdgcn_perm(w4[g][1], s4[g][3], 0x07060100u), lb[g][3]};
    }
  }
  if (tid < NB) cellinfo[tid * NP + (p & 7) * 32 + (p >> 3)] = s_tab[KW * NB + tid];
  __syncthreads();
  {
    const u32 ntot = (u32)__builtin_amdgcn_readfirstlane((int)s_ntot);
    const __amdgpu_buffer_rsrc_t ro = mk_rsrc(part + (size_t)p * CH3, ntot * 16u);
#pragma unroll
    for (int k = 0; k < (MAXT3 + PT - 1) / PT; k++) {
      if ((k + 1) * PT <= 1024 || (u32)(k * PT + wid * 64) < ntot) {
        const u32x2* ep = s_e + (k * PT + tid) * 3;
        const u32x2 a = ep[0], b = ep[1], c = ep[2];
        const u32 l3 = __builtin_amdgcn_perm(c[1], __builtin_amdgcn_perm(b[1], a[1], 0x0c0c0400u), 0x0c040100u);
        __builtin_amdgcn_raw_buffer_store_b128(u32x4{a[0], b[0], c[0], l3}, ro, (k * PT + tid) * 16, 0, SC1);
      }
    }
  }
  if (p * PT < 12288) wfrag[p * PT + tid] = (unsigned short)wval;
  if (p >= NP - BAR_S * BAR_R && tid == 0) bar[(p - (NP - BAR_S * BAR_R)) * BAR_STR] = 0;
}

extern "C" __global__ __launch_bounds__(TPB, 4) void k_cheb(const unsigned short* __restrict__ x, unsigned short* __restrict__ t1,
                                                         const u32x4* __restrict__ part, const u32* __restrict__ cellinfo,
                                                         const unsigned short* __restrict__ wfrag, u32* __restrict__ bar,
                                                         unsigned short* __restrict__ out, int N, int CH3, u32 npb, u32 phm) {
  extern __shared__ __attribute__((aligned(16))) char smem[];
  const int tid = threadIdx.x, lane = tid & 63;
  const int b = (blockIdx.x & 7) * 32 + (blockIdx.x >> 3);
  const int wv = __builtin_amdgcn_readfirstlane(tid >> 6);
  asm volatile("" :: "s"(x), "s"(t1), "s"(part), "s"(cellinfo), "s"(wfrag), "s"(bar), "s"(out), "s"(N), "s"(CH3), "s"(npb), "s"(phm));
  Ctx cx;
  if (wv < 8) {
    cx.ci = ld16_asm(mk_srd(cellinfo, NB * NP * 4), (u32)(b * NP * 4 + lane * 16));
  } else {
    const u32x4 sw = mk_srd(wfrag, 24 * 1024);
    const u32 o = (wv - 8) * 3072;
    dma16x2<false>(sw, L_WFRAG + o, o + lane * 16, o + 1024 + lane * 16);
    dma16(sw, L_WFRAG + o + 2048, o + 2048 + lane * 16);
  }
  __builtin_amdgcn_sched_barrier(0);
  const u32 rowbytes = (u32)N * 128u;
  cx.sp = mk_srd(part, (u32)NP * (u32)CH3 * 16u);
  cx.sx = mk_srd(x, rowbytes);
  cx.st = mk_srd(t1, rowbytes);
  cx.b = b; cx.tid = tid; cx.wv = wv; cx.lane = lane; cx.N = (u32)N; cx.npb = npb; cx.phm = phm;

  f32x4 acc[4];
#pragma unroll
  for (int t = 0; t < 4; t++) acc[t] = f32x4{0.f, 0.f, 0.f, 0.f};

  int R = RND, T3, nw;
  const bool fast = build_lists<true>(smem, cx, 0, R, T3, nw) && T3 <= R;
  u32 vr0, vr1;
  {
    const u32 g = lane >> 3;
    const u32 l0 = g * 16 + wv;
    const u32 o = (((u32)b * npb + l0) << 7) | (((lane & 7) ^ (g & 6u)) << 4);
    vr0 = l0 < npb ? o : OOB;
    vr1 = l0 + 128 < npb ? o + 128 * 128 : OOB;
  }
  const u32 ring0 = L_RING + wv * RU * 2048;
  const u32 xs = fast ? (((nw + 15) >> 4) & 1) * 2048 : 0;
  char* slotx = smem + ring0 + xs;
  char* slot0 = smem + ring0 + (xs ^ 2048);
  const u32 fo = (lane & 15) * 128 + ((((lane >> 4) * 2) ^ (lane & 6u)) << 4);
  gather_accum<false, true>(acc, smem, fast ? nw : 0, cx.sx, wv, lane, vr0, vr1, [] {});
  if (__builtin_expect(!fast, 0)) {
    WAIT_VM(0);
    slow_rounds<false>(acc, smem, cx, R);
    dma16x2<false>(cx.sx, ring0, vr0, vr1);
  }
  u32x4 tf[2];
  pack_rows(acc, tf);
  store_rows<SC1>(slot0, fo, tf, mk_rsrc(t1, rowbytes), vr0, vr1, lane);
  WAIT_VM(0);
  u32 old = 0;
  if (lane == 0) old = atomicAdd((u32*)(smem + L_MISC + 12), 1u);
  const bool last = __builtin_amdgcn_readfirstlane(old) == WPB - 1;
  if (last && lane < BAR_R)
    __hip_atomic_fetch_add(bar + (lane * BAR_S + ((blockIdx.x >> 3) & (BAR_S - 1))) * BAR_STR, 1u, __ATOMIC_RELAXED, __HIP_MEMORY_SCOPE_AGENT);
  dma4(cx.st, L_DUMP, lane * 16 + wv < npb ? ((u32)b * npb + lane * 16 + wv) << 7 : OOB);
  u32x4 xf[2];
  xf[0] = *(const u32x4*)(slotx + fo); xf[1] = *(const u32x4*)(slotx + fo + 16);
  f32x4 o[4];
#pragma unroll
  for (int u = 0; u < 4; u++) { o[u] = f32x4{0.f, 0.f, 0.f, 0.f}; acc[u] = f32x4{0.f, 0.f, 0.f, 0.f}; }
  const char* wl = smem + L_WFRAG + lane * 16;
  dense_part(o, wl, 0, xf);
  dense_part(o, wl, 1, tf);
  asm volatile("" : "+v"(o[0]), "+v"(o[1]), "+v"(o[2]), "+v"(o[3]));
  gather_accum<true, false>(acc, smem, fast ? nw : 0, cx.st, wv, lane, 0, 0, [&] {
    if (last) {
      bool open = lane < BAR_S;
#pragma clang loop unroll(disable)
      for (u32 spins = 0; spins < (1u << 21); spins++) {
        if (open)
          open = __hip_atomic_load(bar + ((blockIdx.x & (BAR_R - 1)) * BAR_S + lane) * BAR_STR, __ATOMIC_RELAXED, __HIP_MEMORY_SCOPE_AGENT) < (u32)(NB / BAR_S);
        const int left = __popcll(__ballot(open));
        if (left == 0) break;
        if (left > BAR_S / 2) __builtin_amdgcn_s_sleep(6); else __builtin_amdgcn_s_sleep(1);
      }
    }
    asm volatile("s_waitcnt lgkmcnt(0)\n\ts_barrier" ::: "memory");
  });
  if (__builtin_expect(!fast, 0)) slow_rounds<true>(acc, smem, cx, R);
  WAIT_VM(0);
  u32x4 t2[2];
#pragma unroll
  for (int s = 0; s < 2; s++) {
    t2[s][0] = pk_bf16(2.f * acc[2 * s][0] - bf_lo(xf[s][0]), 2.f * acc[2 * s][1] - bf_hi(xf[s][0]));
    t2[s][1] = pk_bf16(2.f * acc[2 * s][2] - bf_lo(xf[s][1]), 2.f * acc[2 * s][3] - bf_hi(xf[s][1]));
    t2[s][2] = pk_bf16(2.f * acc[2 * s + 1][0] - bf_lo(xf[s][2]), 2.f * acc[2 * s + 1][1] - bf_hi(xf[s][2]));
    t2[s][3] = pk_bf16(2.f * acc[2 * s + 1][2] - bf_lo(xf[s][3]), 2.f * acc[2 * s + 1][3] - bf_hi(xf[s][3]));
  }
  dense_part(o, wl, 2, t2);
  u32x4 of[2];
  pack_rows(o, of);
  store_rows<SC1>(slot0, fo, of, mk_rsrc(out, rowbytes), vr0, vr1, lane);
}

extern "C" __device__ __attribute__((used, noinline)) void k_text_end() {}

extern "C" void kernel_launch(void* const* d_in, const int* in_sizes, int n_in, void* d_out, int out_size, void* d_ws,
                              size_t ws_size, hipStream_t stream) {
  const unsigned short* x = (const unsigned short*)d_in[0];
  const int* src = (const int*)d_in[1];
  const int* dst = (const int*)d_in[2];
  const unsigned short* ew = (const unsigned short*)d_in[3];
  const unsigned short* w = (const unsigned short*)d_in[4];
  const int N = in_sizes[0] / 64;
  const int E = in_sizes[1];
  int CH = (E + NP - 1) / NP;
  CH = (CH + 3) & ~3;
  const int CH3 = ((CH + 2 * NB) / 3 + 8) & ~7;
  const u32 npb = (u32)((N + NB - 1) / NB);
  const u32 magic = ((1u << 24) + npb - 1) / npb;
  const u32 phm = (1u << 24) / (32u * npb);

  char* ws = (char*)d_ws;
  size_t off = 0;
  u32x4* part = (u32x4*)(ws + off); off += ((size_t)NP * CH3 * 16 + 255) & ~(size_t)255;
  u32* cellinfo = (u32*)(ws + off); off += (size_t)NB * NP * 4;
  unsigned short* t1 = (unsigned short*)(ws + off); off += ((size_t)N * 128 + 255) & ~(size_t)255;
  unsigned short* wfrag = (unsigned short*)(ws + off); off += 24 * 1024;
  u32* bar = (u32*)(ws + off); off += BAR_S * BAR_R * BAR_STR * 4;

  (void)hipFuncSetAttribute((const void*)k_cheb, hipFuncAttributeMaxDynamicSharedMemorySize, L_TOTAL);
  k_part<<<NP, PT, 0, stream>>>(src, dst, ew, w, part, cellinfo, wfrag, bar, E, CH, CH3, npb, magic);
  k_cheb<<<NB, TPB, L_TOTAL, stream>>>(x, t1, part, cellinfo, wfrag, bar, (unsigned short*)d_out, N, CH3, npb, phm);
}
```

```cpp
#include <hip/hip_runtime.h>
#include <hip/hip_bf16.h>
#include <stdint.h>
#include <type_traits>

typedef unsigned u32;
typedef u32 u32x4 __attribute__((ext_vector_type(4)));
typedef u32 u32x2 __attribute__((ext_vector_type(2)));
typedef float f32x4 __attribute__((ext_vector_type(4)));
typedef float f32x2 __attribute__((ext_vector_type(2)));
typedef __bf16 bf16x8 __attribute__((ext_vector_type(8)));
typedef __bf16 bf16x2 __attribute__((ext_vector_type(2)));
typedef short s16x4 __attribute__((ext_vector_type(4)));
typedef __attribute__((address_space(3))) s16x4* lds_s16x4ptr;

constexpr int NB = 256;
constexpr int NP = 256;
constexpr int WPB = 16;
constexpr int TPB = WPB * 64;
constexpr int NPH = 8;
constexpr int MAXCH = 4096;
constexpr int MAXT3 = 1536;
constexpr int RND = 1536;
constexpr int LCAP = 320;
constexpr int KW = 8;
constexpr int PT = KW * 64;
constexpr int PG = 16 / KW;
constexpr int RU = 2;
constexpr int PF0 = 2;
constexpr int PF = 4;
constexpr int BAR_S = 16;
constexpr int BAR_R = 8;
constexpr int BAR_STR = 64;
constexpr u32 OOB = 0x80000000u;
constexpr int SC1 = 16;

constexpr int L_B = 128;
constexpr int L_A = 5376;
constexpr int L_MISC = L_A + WPB * LCAP * 4;
constexpr int L_DUMP = L_MISC + 256;
constexpr int L_RING = 26624;
constexpr int L_MAP = L_RING;
constexpr int L_CNT = L_RING + RND * 4;
constexpr int L_WFRAG = L_RING + WPB * RU * 2048;
constexpr int L_TOTAL = L_WFRAG + 24 * 1024;
static_assert(L_B + WPB * LCAP <= L_A && L_RING % 2048 == 0 && L_DUMP + 256 <= L_RING, "LDS map");
static_assert(L_TOTAL <= 163840 && L_TOTAL > 81920, "exactly one workgroup per CU");

__device__ __forceinline__ __amdgpu_buffer_rsrc_t mk_rsrc(const void* p, u32 bytes) {
  return __builtin_amdgcn_make_buffer_rsrc((void*)p, 0, bytes, 0x00020000);
}
__device__ __forceinline__ u32x4 mk_srd(const void* p, u32 bytes) {
  const unsigned long long a = (unsigned long long)p;
  u32x4 r;
  r[0] = __builtin_amdgcn_readfirstlane((u32)a);
  r[1] = __builtin_amdgcn_readfirstlane((u32)(a >> 32) & 0xffffu);
  r[2] = __builtin_amdgcn_readfirstlane(bytes);
  r[3] = 0x00020000u;
  return r;
}
__device__ __forceinline__ void dma16(u32x4 srd, u32 ldsaddr, u32 voff) {
  u32 keep;
  asm volatile("s_mov_b32 %0, m0\n\ts_mov_b32 m0, %1\n\ts_nop 0\n\tbuffer_load_dwordx4 %2, %3, 0 offen lds\n\ts_mov_b32 m0, %0"
               : "=&s"(keep) : "s"(ldsaddr), "v"(voff), "s"(srd) : "memory");
}
template <bool DEV>
__device__ __forceinline__ void dma16x2(u32x4 srd, u32 ldsaddr, u32 voff0, u32 voff1) {
  u32 keep;
  if constexpr (DEV)
    asm volatile("s_mov_b32 %0, m0\n\ts_mov_b32 m0, %1\n\ts_nop 0\n\tbuffer_load_dwordx4 %2, %4, 0 offen sc1 lds\n\t"
                 "s_add_u32 m0, %1, 0x400\n\ts_nop 0\n\tbuffer_load_dwordx4 %3, %4, 0 offen sc1 lds\n\ts_mov_b32 m0, %0"
                 : "=&s"(keep) : "s"(ldsaddr), "v"(voff0), "v"(voff1), "s"(srd) : "memory", "scc");
  else
    asm volatile("s_mov_b32 %0, m0\n\ts_mov_b32 m0, %1\n\ts_nop 0\n\tbuffer_load_dwordx4 %2, %4, 0 offen lds\n\t"
                 "s_add_u32 m0, %1, 0x400\n\ts_nop 0\n\tbuffer_load_dwordx4 %3, %4, 0 offen lds\n\ts_mov_b32 m0, %0"
                 : "=&s"(keep) : "s"(ldsaddr), "v"(voff0), "v"(voff1), "s"(srd) : "memory", "scc");
}
__device__ __forceinline__ void dma4(u32x4 srd, u32 ldsaddr, u32 voff) {
  u32 keep;
  asm volatile("s_mov_b32 %0, m0\n\ts_mov_b32 m0, %1\n\ts_nop 0\n\tbuffer_load_dword %2, %3, 0 offen sc1 lds\n\ts_mov_b32 m0, %0"
               : "=&s"(keep) : "s"(ldsaddr), "v"(voff), "s"(srd) : "memory");
}
__device__ __forceinline__ u32x4 ld16_asm(u32x4 srd, u32 voff) {
  u32x4 r;
  asm volatile("buffer_load_dwordx4 %0, %1, %2, 0 offen" : "=v"(r) : "v"(voff), "s"(srd) : "memory");
  return r;
}
#define WAIT_VM(n) asm volatile("s_waitcnt vmcnt(" #n ")" ::: "memory")
template <int N_> __device__ __forceinline__ void wait_vm() { asm volatile("s_waitcnt vmcnt(%0)" :: "n"(N_) : "memory"); }

__device__ __forceinline__ u32 pk_bf16(float a, float b) {
  f32x2 v = {a, b};
  return __builtin_bit_cast(u32, __builtin_convertvector(v, bf16x2));
}
__device__ __forceinline__ float bf_lo(u32 v) { return __uint_as_float(v << 16); }
__device__ __forceinline__ float bf_hi(u32 v) { return __uint_as_float(v & 0xffff0000u); }

__device__ __forceinline__ u32 wave_scan_incl(u32 v) {
  asm volatile("s_nop 1\n\tv_add_u32_dpp %0, %0, %0 row_shr:1 row_mask:0xf bank_mask:0xf bound_ctrl:0\n\t"
      "s_nop 1\n\tv_add_u32_dpp %0, %0, %0 row_shr:2 row_mask:0xf bank_mask:0xf bound_ctrl:0\n\t"
      "s_nop 1\n\tv_add_u32_dpp %0, %0, %0 row_shr:4 row_mask:0xf bank_mask:0xf bound_ctrl:0\n\t"
      "s_nop 1\n\tv_add_u32_dpp %0, %0, %0 row_shr:8 row_mask:0xf bank_mask:0xf bound_ctrl:0\n\t"
      "s_nop 1\n\tv_add_u32_dpp %0, %0, %0 row_bcast:15 row_mask:0xa bank_mask:0xf\n\t"
      "s_nop 1\n\tv_add_u32_dpp %0, %0, %0 row_bcast:31 row_mask:0xc bank_mask:0xf\n\ts_nop 1"
      : "+v"(v));
  return v;
}
__device__ __forceinline__ u32 mulhi24(u32 a, u32 b) {
  u32 r; asm("v_mul_hi_u32_u24 %0, %1, %2" : "=v"(r) : "s"(b), "v"(a)); return r;
}
__device__ __forceinline__ u32 madi24(u32 a, int b, u32 c) {
  u32 r; asm("v_mad_i32_i24 %0, %1, %2, %3" : "=v"(r) : "v"(a), "s"(b), "v"(c)); return r;
}
__device__ __forceinline__ u32 div3(u32 r) { return __umul24(r, 0xAAABu) >> 17; }

template <bool PASS2, bool XROWS, class PRE>
__device__ __forceinline__ void gather_accum(f32x4 (&acc)[4], char* smem, int n, u32x4 srd, int wv, int lane,
                                             u32 vr0, u32 vr1, PRE&& before_first_load) {
  constexpr int DIR = PASS2 ? -1 : 1;
  const int nu = (n + 15) >> 4;
  const u32 g = lane >> 3, c = lane & 7, q = lane >> 4;
  const u32 loff = (c ^ (g & 6u)) << 4;
  const u32 ring0 = (u32)(L_RING + wv * RU * 2048);
  const u32 tr0 = ring0 + ((u32)(lane & 60) << 5) + ((u32)(lane & 24) << 2) + (u32)(lane & 3) * 8;
  const u32 tra[4] = {tr0, tr0 ^ 32u, tr0 ^ 64u, tr0 ^ 96u};
  const u32 nloc = (lane & 15) * 16 + wv;
  constexpr int BIAS = PASS2 ? (2 * RU - 1) : 0;
  const int first = wv * LCAP + (PASS2 ? (nu - 1) * 16 : 0) - BIAS * 16;
  const char* sA = smem + L_A + first * 4 + g * 4;
  const char* eA = smem + L_A + first * 4 + q * 16;
  const char* eB = smem + L_B + first + q * 4;
  {
    u32 s0[RU], s1[RU];
#pragma unroll
    for (int u = 0; u < RU; u++) {
      s0[u] = (*(const unsigned short*)(sA + (BIAS + DIR * u) * 64) << 7) | loff;
      s1[u] = (*(const unsigned short*)(sA + (BIAS + DIR * u) * 64 + 32) << 7) | loff;
    }
    if (PASS2) asm volatile("" : "+v"(s0[0]), "+v"(s1[0]), "+v"(s0[1]), "+v"(s1[1]));
    before_first_load();
    __builtin_amdgcn_s_setprio(3);
#pragma unroll
    for (int u = 0; u < RU; u++)
      if (u < nu) dma16x2<PASS2>(srd, ring0 + u * 2048, s0[u], s1[u]);
      else if (XROWS && u == nu) dma16x2<false>(srd, ring0 + u * 2048, vr0, vr1);
  }
  int u = 0;
  auto step = [&](auto SLOT) {
    constexpr int S_ = decltype(SLOT)::value;
    {
      const int xr = 2 * (nu - u - RU + (wv >> 2));
      if (xr < 8) __builtin_amdgcn_s_setprio(0);
      else if (xr < 15) __builtin_amdgcn_s_setprio(1);
      else if (xr < 22) __builtin_amdgcn_s_setprio(2);
    }
    const u32x4 ea = *(const u32x4*)(eA + (BIAS + DIR * S_) * 64);
    const u32 eb = *(const u32*)(eB + (BIAS + DIR * S_) * 16);
    const u32 s0 = *(const unsigned short*)(sA + (BIAS + DIR * (S_ + RU)) * 64);
    const u32 s1 = *(const unsigned short*)(sA + (BIAS + DIR * (S_ + RU)) * 64 + 32);
    __builtin_amdgcn_sched_barrier(0);
    if (XROWS || u + 1 < nu) WAIT_VM(2); else WAIT_VM(0);
    s16x4 a[4];
#pragma unroll
    for (int t = 0; t < 4; t++) a[t] = __builtin_amdgcn_ds_read_tr16_b64_v4i16((lds_s16x4ptr)(uintptr_t)(tra[t] + S_ * 2048));
    __builtin_amdgcn_sched_barrier(0);
    u32 sv[4];
#pragma unroll
    for (int j = 0; j < 4; j++) sv[j] = (((eb >> (8 * j)) & 0xffu) == nloc) ? ea[j] : 0u;
    u32x2 S;
    S[0] = __builtin_amdgcn_perm(sv[1], sv[0], 0x07060302u);
    S[1] = __builtin_amdgcn_perm(sv[3], sv[2], 0x07060302u);
    u32 v0 = (s0 << 7) | loff, v1 = (s1 << 7) | loff;
    if (XROWS && u + RU == nu) { v0 = vr0; v1 = vr1; }
    asm volatile("s_waitcnt lgkmcnt(0)" : "+v"(a[0]), "+v"(a[1]), "+v"(a[2]), "+v"(a[3]), "+v"(S), "+v"(v0), "+v"(v1) :: "memory");
    if (u + RU < nu + (XROWS ? 1 : 0)) dma16x2<PASS2>(srd, ring0 + S_ * 2048, v0, v1);
    __builtin_amdgcn_sched_barrier(0);
#pragma unroll
    for (int t = 0; t < 4; t++)
      acc[t] = __builtin_amdgcn_mfma_f32_16x16x16bf16_1k(a[t], __builtin_bit_cast(s16x4, S), acc[t], 0, 0, 0);
    u++;
  };
#pragma clang loop unroll(disable)
  while (u < nu) {
    step(std::integral_constant<int, 0>{});
    if (u >= nu) break;
    step(std::integral_constant<int, 1>{});
    sA += DIR * RU * 64; eA += DIR * RU * 64; eB += DIR * RU * 16;
  }
  __builtin_amdgcn_s_setprio(0);
}

struct Ctx {
  u32x4 sp, sx, st;
  u32x4 ci;
  int b, tid, wv, lane;
  u32 N, npb, phm;
};

template <bool FIRST>
__device__ __forceinline__ bool build_lists(char* smem, Ctx& cx, int r0, int& R, int& T3, int& nw) {
  const int tid = cx.tid, wv = cx.wv, lane = cx.lane;
  u32* s_map = (u32*)(smem + L_MAP);
  char* s_cnt = smem + L_CNT + wv * 4;
  const u32 rot = 7u - (u32)(cx.b >> 5);
  char* lA = smem + L_A;
  char* lB = smem + L_B;
  if (!FIRST || wv < 8) WAIT_VM(0);
  asm volatile("" : "+v"(cx.ci) :: "memory");
  if (wv < 8) {
    const u32x4 ci = cx.ci;
    u32 len[4], ntr[4];
#pragma unroll
    for (int k = 0; k < 4; k++) { len[k] = ci[k] >> 19; ntr[k] = div3(len[k] + 2); }
    const u32 tsum = ntr[0] + ntr[1] + ntr[2] + ntr[3];
    const u32 tinc = wave_scan_incl(tsum);
    if (tid == 63) *(u32*)(smem + L_MISC) = tinc;
    int pre = (int)(tinc - tsum) - r0;
    if (FIRST && __builtin_expect(__builtin_amdgcn_readlane((int)tinc, 63) <= R, 1)) {
      const u32 mx = max(max(ntr[0], ntr[1]), max(ntr[2], ntr[3]));
      u32 j = wv;
#pragma clang loop unroll(disable)
      do {
        u32 pk = pre + j;
#pragma unroll
        for (int k = 0; k < 4; k++) {
          if (j < ntr[k]) s_map[pk] = ((ci[k] & 0x7ffffu) + j) | (min(3u, len[k] - 3u * j) << 30);
          pk += ntr[k];
        }
        j += 8;
      } while (__any(j < mx));
    } else {
#pragma unroll
      for (int k = 0; k < 4; k++) {
        const int jh = min((int)ntr[k], R - pre);
#pragma clang loop unroll(disable) vectorize(disable)
        for (int j = max(0, -pre) + wv; j < jh; j += 8)
          s_map[pre + j] = ((ci[k] & 0x7ffffu) + j) | (min(3u, len[k] - 3u * j) << 30);
        pre += ntr[k];
      }
    }
  } else {
    *(u32x4*)(smem + L_CNT + (tid - 512) * 16) = u32x4{0, 0, 0, 0};
    if (tid < 512 + 32) *(u32x4*)(smem + L_CNT + tid * 16) = u32x4{0, 0, 0, 0};
    if (tid == 512) {
      *(u32*)(smem + L_MISC + 8) = 0;
      if (FIRST) *(u32*)(smem + L_MISC + 12) = 0;
    }
  }
  __syncthreads();
  u32 t3v = *(const u32*)(smem + L_MISC), m0 = s_map[tid], m1 = s_map[tid + TPB];
  asm volatile("" : "+v"(t3v), "+v"(m0), "+v"(m1));
  T3 = __builtin_amdgcn_readfirstlane(t3v);
  const int nf = min(T3 - r0, R);
  const bool two = wv * 64 + TPB < nf;
  m0 = (tid < nf) ? m0 : (OOB >> 4);
  m1 = (tid + TPB < nf) ? m1 : (OOB >> 4);
  u32x4 e0 = ld16_asm(cx.sp, m0 << 4);
  u32x4 e1;
  if (two) e1 = ld16_asm(cx.sp, m1 << 4);
  if (PF > 0 && FIRST) {
    __builtin_amdgcn_s_barrier();
    if (wv >= WPB - PF) {
      u32 l = (((u32)(cx.b >> 5) + 1u) & 7u) * 32u * cx.npb + (u32)(PF0 * 64 + TPB - 1 - tid) * 32u + (u32)(cx.b & 31);
      l = l >= cx.N ? l - cx.N : l;
      dma4(cx.sx, L_DUMP, l << 7);
      WAIT_VM(1);
    } else {
      WAIT_VM(0);
    }
  } else {
    WAIT_VM(0);
  }
  asm volatile("" : "+v"(e0), "+v"(e1) :: "memory");
  auto cadr = [&](u32 lo, u32 lb) {
    return __umul24(((lb & 15u) << 3) | (((__umul24(lo & 0xffffu, cx.phm) >> 24) + rot) & 7u), 68u);
  };
  const u32 ca0 = cadr(e0[0], e0[3]), ca1 = cadr(e0[1], e0[3] >> 8), ca2 = cadr(e0[2], e0[3] >> 16);
  u32 rk0 = 0, rk1 = 0, rk2 = 0, rk3 = 0, rk4 = 0, rk5 = 0, ca3 = 0, ca4 = 0, ca5 = 0;
  const u32 v0 = m0 >> 30, v1 = m1 >> 30;
  if (v0 > 0) rk0 = atomicAdd((u32*)(s_cnt + ca0), 1u);
  asm volatile("" ::: "memory");
  if (v0 > 1) rk1 = atomicAdd((u32*)(s_cnt + ca1), 1u);
  asm volatile("" ::: "memory");
  if (v0 > 2) rk2 = atomicAdd((u32*)(s_cnt + ca2), 1u);
  if (two) {
    ca3 = cadr(e1[0], e1[3]); ca4 = cadr(e1[1], e1[3] >> 8); ca5 = cadr(e1[2], e1[3] >> 16);
    if (v1 > 0) rk3 = atomicAdd((u32*)(s_cnt + ca3), 1u);
    asm volatile("" ::: "memory");
    if (v1 > 1) rk4 = atomicAdd((u32*)(s_cnt + ca4), 1u);
    asm volatile("" ::: "memory");
    if (v1 > 2) rk5 = atomicAdd((u32*)(s_cnt + ca5), 1u);
  }
  __syncthreads();
  {
    const int ws = wv * LCAP;
    u32* cp = (u32*)(smem + L_CNT) + (wv * NPH + (lane >> 3)) * 17 + (lane & 7) * 2;
    const u32 c0 = cp[0], c1 = cp[1];
    const u32 t = c0 + c1;
    const u32 inc = wave_scan_incl(t);
    const u32 ex = inc - t + ws;
    cp[0] = ex; cp[1] = ex + c0;
    nw = __builtin_amdgcn_readlane((int)inc, 63);
    if (nw > LCAP) {
      if (lane == 0) *(u32*)(smem + L_MISC + 8) = 1;
    } else if (nw + lane < ((nw + 15) & ~15)) {
      *(u32*)(lA + (ws + nw + lane) * 4) = 0x0000ffffu;
      *(unsigned char*)(lB + ws + nw + lane) = 0xffu;
    }
  }
  __syncthreads();
  u32 p0 = *(const u32*)(s_cnt + ca0), p1 = *(const u32*)(s_cnt + ca1), p2 = *(const u32*)(s_cnt + ca2);
  u32 p3 = 0, p4 = 0, p5 = 0;
  if (two) { p3 = *(const u32*)(s_cnt + ca3); p4 = *(const u32*)(s_cnt + ca4); p5 = *(const u32*)(s_cnt + ca5); }
  u32 ovf = *(const u32*)(smem + L_MISC + 8);
  asm volatile("" : "+v"(p0), "+v"(p1), "+v"(p2), "+v"(p3), "+v"(p4), "+v"(p5), "+v"(ovf));
  if (__builtin_expect(__builtin_amdgcn_readfirstlane(ovf) != 0, 0)) {
    R = max(96, min(R, nf) >> 1);
    __syncthreads();
    return false;
  }
  auto put = [&](u32 pos, u32 lo, u32 lb) {
    *(u32*)(lA + pos * 4) = lo;
    *(unsigned char*)(lB + pos) = (unsigned char)lb;
  };
  if (v0 > 0) put(p0 + rk0, e0[0], e0[3]);
  if (v0 > 1) put(p1 + rk1, e0[1], e0[3] >> 8);
  if (v0 > 2) put(p2 + rk2, e0[2], e0[3] >> 16);
  if (two) {
    if (v1 > 0) put(p3 + rk3, e1[0], e1[3]);
    if (v1 > 1) put(p4 + rk4, e1[1], e1[3] >> 8);
    if (v1 > 2) put(p5 + rk5, e1[2], e1[3] >> 16);
  }
  __syncthreads();
  return true;
}

template <bool PASS2>
__device__ __forceinline__ void slow_rounds(f32x4 (&acc)[4], char* smem, Ctx& cx, int R) {
  int r0 = 0, T3 = 1, nw = 0;
#pragma clang loop unroll(disable)
  while (r0 < T3) {
    __syncthreads();
    if (!build_lists<false>(smem, cx, r0, R, T3, nw)) continue;
    gather_accum<PASS2, false>(acc, smem, nw, PASS2 ? cx.st : cx.sx, cx.wv, cx.lane, 0, 0, [] {});
    r0 += R;
  }
}

__device__ __forceinline__ void dense_part(f32x4 (&o)[4], const char* wl, int k, const u32x4 (&bf)[2]) {
#pragma unroll
  for (int s = 0; s < 2; s++) {
#pragma unroll
    for (int u = 0; u < 4; u++) {
      const u32x4 af = *(const u32x4*)(wl + ((k * 2 + s) * 4 + u) * 1024);
      o[u] = __builtin_amdgcn_mfma_f32_16x16x32_bf16(__builtin_bit_cast(bf16x8, af), __builtin_bit_cast(bf16x8, bf[s]), o[u], 0, 0, 0);
    }
  }
}
__device__ __forceinline__ void pack_rows(const f32x4 (&a)[4], u32x4 (&o)[2]) {
  o[0] = u32x4{pk_bf16(a[0][0], a[0][1]), pk_bf16(a[0][2], a[0][3]), pk_bf16(a[1][0], a[1][1]), pk_bf16(a[1][2], a[1][3])};
  o[1] = u32x4{pk_bf16(a[2][0], a[2][1]), pk_bf16(a[2][2], a[2][3]), pk_bf16(a[3][0], a[3][1]), pk_bf16(a[3][2], a[3][3])};
}
template <int AUX>
__device__ __forceinline__ void store_rows(char* slot, u32 fo, const u32x4 (&o)[2], __amdgpu_buffer_rsrc_t r, u32 vr0, u32 vr1,
                                           int lane) {
  *(u32x4*)(slot + fo) = o[0];
  *(u32x4*)(slot + fo + 16) = o[1];
  asm volatile("" ::: "memory");
  const u32x4 r0 = *(const u32x4*)(slot + lane * 16);
  const u32x4 r1 = *(const u32x4*)(slot + 1024 + lane * 16);
  __builtin_amdgcn_raw_buffer_store_b128(r0, r, vr0, 0, AUX);
  __builtin_amdgcn_raw_buffer_store_b128(r1, r, vr1, 0, AUX);
}

extern "C" __global__ __launch_bounds__(PT) void k_part(const int* __restrict__ src, const int* __restrict__ dst,
                                                        const unsigned short* __restrict__ ew, const unsigned short* __restrict__ w,
                                                        u32x4* __restrict__ part, u32* __restrict__ cellinfo,
                                                        unsigned short* __restrict__ wfrag, u32* __restrict__ bar,
                                                        int E, int CH, int CH3, u32 npb, u32 magic) {
  __shared__ __attribute__((aligned(16))) u32 s_tab[(KW + 1) * NB];
  __shared__ __attribute__((aligned(16))) u32x2 s_e[MAXT3 * 3];
  __shared__ u32 s_ntot;
  const int tid = threadIdx.x, p = blockIdx.x, lane = tid & 63;
  const int wid = __builtin_amdgcn_readfirstlane(tid >> 6);
  const int e0 = p * CH;
  int n = E - e0; n = n < 0 ? 0 : (n > CH ? CH : n);
  u32x4 d4[PG], s4[PG];
  u32x2 w4[PG];
  u32 wval = 0, cpf = 0;
  {
    u32 eo[PG];
#pragma unroll
    for (int g = 0; g < PG; g++) eo[g] = (g * PT * 4 + tid * 4 < n) ? (u32)(e0 + g * PT * 4 + tid * 4) * 2u : 0x40000000u;
    const u32x4 sd = mk_srd(dst, (u32)E * 4u), ss = mk_srd(src, (u32)E * 4u), sw = mk_srd(ew, ((u32)E * 2u + 3u) & ~3u);
#pragma unroll
    for (int g = 0; g < PG; g++) d4[g] = ld16_asm(sd, eo[g] * 2u);
    {
      unsigned long long pc; u32 off, own, len;
      asm volatile("s_getpc_b64 %0\n.Lcpf_mark:\n\ts_mov_b32 %1, .Lcpf_mark-k_part\n\ts_mov_b32 %2, k_cheb-k_part\n\ts_mov_b32 %3, k_text_end-k_part"
                   : "=s"(pc), "=s"(off), "=s"(own), "=s"(len));
      len = len < 0x40000u ? len : 0u;
      const u32 o = tid < 32 ? tid * 128u : own + ((u32)(p >> 3) * 6u + tid - 32u) * 128u;
      asm volatile("buffer_load_dword %0, %1, %2, 0 offen" : "=v"(cpf) : "v"(tid < 38 ? o : OOB), "s"(mk_srd((const void*)(pc - off), len)) : "memory");
    }
#pragma unroll
    for (int g = 0; g < PG; g++) {
      s4[g] = ld16_asm(ss, eo[g] * 2u);
      asm volatile("buffer_load_dwordx2 %0, %1, %2, 0 offen" : "=v"(w4[g]) : "v"(eo[g]), "s"(sw) : "memory");
    }
    const int o = p * PT + tid;
    const int j = o & 7, l = (o >> 3) & 63, u = (o >> 9) & 3, sx = (o >> 11) & 1, k = o >> 12;
    const int i = l & 15, q = l >> 4;
    const int e = 16 * (i >> 2) + 4 * u + (i & 3);
    const int dd = (k == 1) ? (16 * (2 * sx + (j >> 2)) + 4 * q + (j & 3)) : (16 * q + 8 * sx + j);
    asm volatile("buffer_load_ushort %0, %1, %2, 0 offen" : "=v"(wval) : "v"((k * 4096 + dd * 64 + e) * 2), "s"(mk_srd(w, 3 * 4096 * 2)) : "memory");
  }
  u32* cnt = s_tab + wid * NB;
  *(u32x4*)(cnt + lane * 4) = u32x4{0, 0, 0, 0};
  u32 ha[PG][4], rk[PG][4], lb[PG][4];
#pragma unroll
  for (int g = 0; g < PG; g++) {
    if (g == 0) wait_vm<3 * PG + 1>(); else if (g == 1) wait_vm<3 * PG>(); else if (g == 2) wait_vm<3 * PG - 1>(); else wait_vm<3 * PG - 2>();
    asm volatile("" : "+v"(d4[g]) :: "memory");
#pragma unroll
    for (int k = 0; k < 4; k++) ha[g][k] = mulhi24(d4[g][k] << 8, magic);
    if (g * PT * 4 + tid * 4 < n) {
#pragma unroll
      for (int k = 0; k < 4; k++) rk[g][k] = atomicAdd(&cnt[ha[g][k]], 1u);
#pragma unroll
      for (int k = 0; k < 4; k++) lb[g][k] = madi24(ha[g][k], -(int)npb, d4[g][k]);
    }
  }
  __syncthreads();
  if (wid == KW - 1) {
    u32x4 c[KW];
#pragma unroll
    for (int r = 0; r < KW; r++) c[r] = *(const u32x4*)(s_tab + r * NB + lane * 4);
    u32x4 h = c[0];
#pragma unroll
    for (int r = 1; r < KW; r++) h += c[r];
    const u32 t0 = div3(h[0] + 2), t1 = div3(h[1] + 2), t2 = div3(h[2] + 2), t3 = div3(h[3] + 2);
    const u32 t = t0 + t1 + t2 + t3;
    const u32 inc = wave_scan_incl(t);
    const u32 ex = inc - t;
    const u32x4 bs = {ex, ex + t0, ex + t0 + t1, ex + t0 + t1 + t2};
    u32x4 run = bs * 3u;
#pragma unroll
    for (int r = 0; r < KW; r++) { *(u32x4*)(s_tab + r * NB + lane * 4) = run; run += c[r]; }
    *(u32x4*)(s_tab + KW * NB + lane * 4) = (bs + (u32)p * (u32)CH3) | (h << 19);
    if (lane == 63) s_ntot = inc;
  }
  __syncthreads();
  u32 xo[PG][4];
#pragma unroll
  for (int g = 0; g < PG; g++) {
#pragma unroll
    for (int k = 0; k < 4; k++) xo[g][k] = cnt[ha[g][k]];
  }
  WAIT_VM(0);
#pragma unroll
  for (int g = 0; g < PG; g++) asm volatile("" : "+v"(s4[g]), "+v"(w4[g]) :: "memory");
  asm volatile("" : "+v"(wval), "+v"(cpf) :: "memory");
#pragma unroll
  for (int g = 0; g < PG; g++) {
    const int i0 = g * PT * 4 + tid * 4;
    if (i0 < n) {
      if (i0 + 3 >= n) {
#pragma unroll
        for (int k = 1; k < 4; k++)
          if (i0 + k >= n) { s4[g][k] = 0xffffu; w4[g][k >> 1] &= (k & 1) ? 0x0000ffffu : 0u; }
      }
      s_e[rk[g][0] + xo[g][0]] = u32x2{__builtin_amdgcn_perm(w4[g][0], s4[g][0], 0x05040100u), lb[g][0]};
      s_e[rk[g][1] + xo[g][1]] = u32x2{__builtin_amdgcn_perm(w4[g][0], s4[g][1], 0x07060100u), lb[g][1]};
      s_e[rk[g][2] + xo[g][2]] = u32x2{__builtin_amdgcn_perm(w4[g][1], s4[g][2], 0x05040100u), lb[g][2]};
      s_e[rk[g][3] + xo[g][3]] = u32x2{__builtin_amdgcn_perm(w4[g][1], s4[g][3], 0x07060100u), lb[g][3]};
    }
  }
  if (tid < NB) cellinfo[tid * NP + (p & 7) * 32 + (p >> 3)] = s_tab[KW * NB + tid];
  __syncthreads();
  {
    const u32 ntot = (u32)__builtin_amdgcn_readfirstlane((int)s_ntot);
    const __amdgpu_buffer_rsrc_t ro = mk_rsrc(part + (size_t)p * CH3, ntot * 16u);
#pragma unroll
    for (int k = 0; k < (MAXT3 + PT - 1) / PT; k++) {
      if ((k + 1) * PT <= 1024 || (u32)(k * PT + wid * 64) < ntot) {
        const u32x2* ep = s_e + (k * PT + tid) * 3;
        const u32x2 a = ep[0], b = ep[1], c = ep[2];
        const u32 l3 = __builtin_amdgcn_perm(c[1], __builtin_amdgcn_perm(b[1], a[1], 0x0c0c0400u), 0x0c040100u);
        __builtin_amdgcn_raw_buffer_store_b128(u32x4{a[0], b[0], c[0], l3}, ro, (k * PT + tid) * 16, 0, SC1);
      }
    }
  }
  if (p * PT < 12288) wfrag[p * PT + tid] = (unsigned short)wval;
  if (p >= NP - BAR_S * BAR_R && tid == 0) bar[(p - (NP - BAR_S * BAR_R)) * BAR_STR] = 0;
}

extern "C" __global__ __launch_bounds__(TPB, 4) void k_cheb(const unsigned short* __restrict__ x, unsigned short* __restrict__ t1,
                                                         const u32x4* __restrict__ part, const u32* __restrict__ cellinfo,
                                                         const unsigned short* __restrict__ wfrag, u32* __restrict__ bar,
                                                         unsigned short* __restrict__ out, int N, int CH3, u32 npb, u32 phm) {
  extern __shared__ __attribute__((aligned(16))) char smem[];
  const int tid = threadIdx.x, lane = tid & 63;
  const int b = (blockIdx.x & 7) * 32 + (blockIdx.x >> 3);
  const int wv = __builtin_amdgcn_readfirstlane(tid >> 6);
  asm volatile("" :: "s"(x), "s"(t1), "s"(part), "s"(cellinfo), "s"(wfrag), "s"(bar), "s"(out), "s"(N), "s"(CH3), "s"(npb), "s"(phm));
  Ctx cx;
  if (wv < 8) {
    cx.ci = ld16_asm(mk_srd(cellinfo, NB * NP * 4), (u32)(b * NP * 4 + lane * 16));
  } else {
    const u32x4 sw = mk_srd(wfrag, 24 * 1024);
    const u32 o = (wv - 8) * 3072;
    dma16x2<false>(sw, L_WFRAG + o, o + lane * 16, o + 1024 + lane * 16);
    dma16(sw, L_WFRAG + o + 2048, o + 2048 + lane * 16);
    if (wv >= WPB - PF0) {
      u32 l = (((u32)(b >> 5) + 1u) & 7u) * 32u * npb + (u32)(TPB - 1 - tid) * 32u + (u32)(b & 31);
      l = l >= (u32)N ? l - (u32)N : l;
      dma4(mk_srd(x, (u32)N * 128u), L_DUMP, l << 7);
    }
  }
  __builtin_amdgcn_sched_barrier(0);
  const u32 rowbytes = (u32)N * 128u;
  cx.sp = mk_srd(part, (u32)NP * (u32)CH3 * 16u);
  cx.sx = mk_srd(x, rowbytes);
  cx.st = mk_srd(t1, rowbytes);
  cx.b = b; cx.tid = tid; cx.wv = wv; cx.lane = lane; cx.N = (u32)N; cx.npb = npb; cx.phm = phm;

  f32x4 acc[4];
#pragma unroll
  for (int t = 0; t < 4; t++) acc[t] = f32x4{0.f, 0.f, 0.f, 0.f};

  int R = RND, T3, nw;
  const bool fast = build_lists<true>(smem, cx, 0, R, T3, nw) && T3 <= R;
  u32 vr0, vr1;
  {
    const u32 g = lane >> 3;
    const u32 l0 = g * 16 + wv;
    const u32 o = (((u32)b * npb + l0) << 7) | (((lane & 7) ^ (g & 6u)) << 4);
    vr0 = l0 < npb ? o : OOB;
    vr1 = l0 + 128 < npb ? o + 128 * 128 : OOB;
  }
  const u32 ring0 = L_RING + wv * RU * 2048;
  const u32 xs = fast ? (((nw + 15) >> 4) & 1) * 2048 : 0;
  char* slotx = smem + ring0 + xs;
  char* slot0 = smem + ring0 + (xs ^ 2048);
  const u32 fo = (lane & 15) * 128 + ((((lane >> 4) * 2) ^ (lane & 6u)) << 4);
  gather_accum<false, true>(acc, smem, fast ? nw : 0, cx.sx, wv, lane, vr0, vr1, [] {});
  if (__builtin_expect(!fast, 0)) {
    WAIT_VM(0);
    slow_rounds<false>(acc, smem, cx, R);
    dma16x2<false>(cx.sx, ring0, vr0, vr1);
  }
  u32x4 tf[2];
  pack_rows(acc, tf);
  store_rows<SC1>(slot0, fo, tf, mk_rsrc(t1, rowbytes), vr0, vr1, lane);
  WAIT_VM(0);
  u32 old = 0;
  if (lane == 0) old = atomicAdd((u32*)(smem + L_MISC + 12), 1u);
  const bool last = __builtin_amdgcn_readfirstlane(old) == WPB - 1;
  if (last && lane < BAR_R)
    __hip_atomic_fetch_add(bar + (lane * BAR_S + ((blockIdx.x >> 3) & (BAR_S - 1))) * BAR_STR, 1u, __ATOMIC_RELAXED, __HIP_MEMORY_SCOPE_AGENT);
  dma4(cx.st, L_DUMP, lane * 16 + wv < npb ? ((u32)b * npb + lane * 16 + wv) << 7 : OOB);
  u32x4 xf[2];
  xf[0] = *(const u32x4*)(slotx + fo); xf[1] = *(const u32x4*)(slotx + fo + 16);
  f32x4 o[4];
#pragma unroll
  for (int u = 0; u < 4; u++) { o[u] = f32x4{0.f, 0.f, 0.f, 0.f}; acc[u] = f32x4{0.f, 0.f, 0.f, 0.f}; }
  const char* wl = smem + L_WFRAG + lane * 16;
  dense_part(o, wl, 0, xf);
  dense_part(o, wl, 1, tf);
  asm volatile("" : "+v"(o[0]), "+v"(o[1]), "+v"(o[2]), "+v"(o[3]));
  gather_accum<true, false>(acc, smem, fast ? nw : 0, cx.st, wv, lane, 0, 0, [&] {
    if (last) {
      bool open = lane < BAR_S;
#pragma clang loop unroll(disable)
      for (u32 spins = 0; spins < (1u << 21); spins++) {
        if (open)
          open = __hip_atomic_load(bar + ((blockIdx.x & (BAR_R - 1)) * BAR_S + lane) * BAR_STR, __ATOMIC_RELAXED, __HIP_MEMORY_SCOPE_AGENT) < (u32)(NB / BAR_S);
        const int left = __popcll(__ballot(open));
        if (left == 0) break;
        if (left > BAR_S / 2) __builtin_amdgcn_s_sleep(6); else __builtin_amdgcn_s_sleep(1);
      }
    }
    asm volatile("s_waitcnt lgkmcnt(0)\n\ts_barrier" ::: "memory");
  });
  if (__builtin_expect(!fast, 0)) slow_rounds<true>(acc, smem, cx, R);
  WAIT_VM(0);
  u32x4 t2[2];
#pragma unroll
  for (int s = 0; s < 2; s++) {
    t2[s][0] = pk_bf16(2.f * acc[2 * s][0] - bf_lo(xf[s][0]), 2.f * acc[2 * s][1] - bf_hi(xf[s][0]));
    t2[s][1] = pk_bf16(2.f * acc[2 * s][2] - bf_lo(xf[s][1]), 2.f * acc[2 * s][3] - bf_hi(xf[s][1]));
    t2[s][2] = pk_bf16(2.f * acc[2 * s + 1][0] - bf_lo(xf[s][2]), 2.f * acc[2 * s + 1][1] - bf_hi(xf[s][2]));
    t2[s][3] = pk_bf16(2.f * acc[2 * s + 1][2] - bf_lo(xf[s][3]), 2.f * acc[2 * s + 1][3] - bf_hi(xf[s][3]));
  }
  dense_part(o, wl, 2, t2);
  u32x4 of[2];
  pack_rows(o, of);
  store_rows<SC1>(slot0, fo, of, mk_rsrc(out, rowbytes), vr0, vr1, lane);
}

extern "C" __device__ __attribute__((used, noinline)) void k_text_end() {}

extern "C" void kernel_launch(void* const* d_in, const int* in_sizes, int n_in, void* d_out, int out_size, void* d_ws,
                              size_t ws_size, hipStream_t stream) {
  const unsigned short* x = (const unsigned short*)d_in[0];
  const int* src = (const int*)d_in[1];
  const int* dst = (const int*)d_in[2];
  const unsigned short* ew = (const unsigned short*)d_in[3];
  const unsigned short* w = (const unsigned short*)d_in[4];
  const int N = in_sizes[0] / 64;
  const int E = in_sizes[1];
  int CH = (E + NP - 1) / NP;
  CH = (CH + 3) & ~3;
  const int CH3 = ((CH + 2 * NB) / 3 + 8) & ~7;
  if (N < 2 * NB || N > 65535 || CH > MAXCH) return;
  const u32 npb = (u32)((N + NB - 1) / NB);
  const u32 magic = ((1u << 24) + npb - 1) / npb;
  const u32 phm = (1u << 24) / (32u * npb);

  char* ws = (char*)d_ws;
  size_t off = 0;
  u32x4* part = (u32x4*)(ws + off); off += ((size_t)NP * CH3 * 16 + 255) & ~(size_t)255;
  u32* cellinfo = (u32*)(ws + off); off += (size_t)NB * NP * 4;
  unsigned short* t1 = (unsigned short*)(ws + off); off += ((size_t)N * 128 + 255) & ~(size_t)255;
  unsigned short* wfrag = (unsigned short*)(ws + off); off += 24 * 1024;
  u32* bar = (u32*)(ws + off); off += BAR_S * BAR_R * BAR_STR * 4;

  (void)hipFuncSetAttribute((const void*)k_cheb, hipFuncAttributeMaxDynamicSharedMemorySize, L_TOTAL);
  k_part<<<NP, PT, 0, stream>>>(src, dst, ew, w, part, cellinfo, wfrag, bar, E, CH, CH3, npb, magic);
  k_cheb<<<NB, TPB, L_TOTAL, stream>>>(x, t1, part, cellinfo, wfrag, bar, (unsigned short*)d_out, N, CH3, npb, phm);
}
```
